```python
import torch

def _softmax(x, axis=-1):
    return torch.softmax(x, dim=axis)
def _gelu(x):
    return torch.nn.functional.gelu(x, approximate="tanh")
def _silu(x):
    return torch.nn.functional.silu(x)
def _sigmoid(x):
    return torch.sigmoid(x)
def _layernorm(x, axis=-1, eps=1e-5):
    m = x.mean(dim=axis, keepdim=True)
    v = x.var(dim=axis, keepdim=True, unbiased=False)
    return (x - m) / torch.sqrt(v + eps)
def _scatter_add(tgt, idx, src, axis=0):
    return tgt.clone().index_add_(axis, idx, src)
def _segment_sum(src, seg, n):
    out = torch.zeros((n,) + src.shape[1:], dtype=src.dtype)
    return out.index_add_(0, seg, src)
def _pad(x, pw):
    flat = tuple(v for pair in reversed(pw) for v in pair)
    return torch.nn.functional.pad(x, flat)

kernel_name = 'standalone__mixer_token_mlp__prompt'

def setup_inputs():

    _w = (lambda a: (lambda t: t.to(torch.bfloat16) if t.is_floating_point() else t)(torch.as_tensor(a)))
    B, S, D, F = 1, 2048, 512, 1792
    return {
        "x": _w((lambda *s: torch.randn(*s, dtype=torch.bfloat16) * 0.05)(B, S, D)),
        "w1": _w((lambda *s: torch.randn(*s, dtype=torch.bfloat16) * 0.2)(S, F) * (S ** -0.5)),
        "b1": _w((lambda *s: torch.randn(*s, dtype=torch.bfloat16) * 3.2)(F) * 0.02),
        "w2": _w((lambda *s: torch.randn(*s, dtype=torch.bfloat16) * 0.05)(F, S) * (F ** -0.5)),
        "b2": _w((lambda *s: torch.randn(*s, dtype=torch.bfloat16) * 0.1)(S) * 0.02),
    }

def reference(x, w1, b1, w2, b2):
    B, S, D = x.shape
    h = _layernorm(x, axis=-1)
    u = torch.einsum("bsd,sf->bdf", h, w1) + b1
    u = _gelu(u)
    y = torch.einsum("bdf,fs->bds", u, w2) + b2
    return (lambda x, *d: x.permute(*d))(y, 0, 2, 1)
```

```cpp
#include <hip/hip_runtime.h>
#include <hip/hip_bf16.h>
#include <stdint.h>

typedef float    f32x16 __attribute__((ext_vector_type(16)));
typedef float    f32x4  __attribute__((ext_vector_type(4)));
typedef float    f32x2  __attribute__((ext_vector_type(2)));
typedef __bf16   bf16x8 __attribute__((ext_vector_type(8)));
typedef __bf16   bf16x4 __attribute__((ext_vector_type(4)));
typedef __bf16   bf16x2 __attribute__((ext_vector_type(2)));
typedef short    s16x4  __attribute__((ext_vector_type(4)));
typedef unsigned u32x4  __attribute__((ext_vector_type(4)));
typedef unsigned u32x2  __attribute__((ext_vector_type(2)));
#define LAS __attribute__((address_space(3)))
#define DI __device__ __forceinline__

constexpr int S = 2048, D = 512, F = 1792;
constexpr int KS1 = S / 16, KS2 = F / 16;

DI unsigned cvtpk(float lo, float hi) { f32x2 v = {lo, hi}; bf16x2 b = __builtin_convertvector(v, bf16x2); return __builtin_bit_cast(unsigned, b); }
DI float bflo(unsigned u) { return __uint_as_float(u << 16); }
DI float bfhi(unsigned u) { return __uint_as_float(u & 0xffff0000u); }
DI float rbf(float x) { return bflo(cvtpk(x, x)); }
DI float wave_sum(float v) {
#pragma unroll
  for (int m = 32; m >= 1; m >>= 1) v += __shfl_xor(v, m);
  return v;
}

__global__ __launch_bounds__(256) void k_ln(const u32x4* __restrict__ x, u32x4* __restrict__ hF) {
  __shared__ __attribute__((aligned(16))) char T[8 * 1088];
  const int b = blockIdx.x, w = threadIdx.x >> 6, l = threadIdx.x & 63;
#pragma unroll
  for (int i = 0; i < 2; ++i) {
    const int r = 2 * w + i;
    u32x4 v = x[(8 * b + r) * 64 + l];
    float e[8];
#pragma unroll
    for (int j = 0; j < 4; ++j) { e[2 * j] = bflo(v[j]); e[2 * j + 1] = bfhi(v[j]); }
    float s = 0;
#pragma unroll
    for (int j = 0; j < 8; ++j) s += e[j];
    const float mean = wave_sum(s) * (1.0f / D);
    float q = 0;
#pragma unroll
    for (int j = 0; j < 8; ++j) { float t = e[j] - mean; q += t * t; }
    const float var = rbf(wave_sum(q) * (1.0f / D));
    const float mb = rbf(mean);
    const float den = rbf(__builtin_sqrtf(rbf(var + 1e-5f)));
    const float rd = 1.0f / den;
    u32x4 o;
#pragma unroll
    for (int j = 0; j < 4; ++j) o[j] = cvtpk(rbf(e[2 * j] - mb) * rd, rbf(e[2 * j + 1] - mb) * rd);
    *(u32x4*)(T + r * 1088 + l * 16) = o;
  }
  __syncthreads();
  const int ks = b >> 1, hh = b & 1;
  const int g = l >> 4, q = (l & 15) >> 2, p = l & 3;
#pragma unroll
  for (int it = 0; it < 2; ++it) {
    const int sg = 4 * w + 2 * it + (l >> 5);
    const int col = 32 * sg + 16 * (g & 1) + 4 * p;
    s16x4 lo = __builtin_amdgcn_ds_read_tr16_b64_v4i16((LAS s16x4*)(T + (q) * 1088 + col * 2));
    s16x4 hi = __builtin_amdgcn_ds_read_tr16_b64_v4i16((LAS s16x4*)(T + (4 + q) * 1088 + col * 2));
    u32x2 a = __builtin_bit_cast(u32x2, lo), c = __builtin_bit_cast(u32x2, hi);
    hF[(((sg >> 1) * KS1 + ks) * 2 + (sg & 1)) * 64 + 32 * hh + (l & 31)] = (u32x4){a[0], a[1], c[0], c[1]};
  }
}

template <int PHASE>
__global__ __launch_bounds__(256, 1) void k_gemm(const u32x4* __restrict__ act, const char* __restrict__ W, const unsigned short* __restrict__ bias,
                                              void* __restrict__ outp) {
  constexpr int NSTEP = PHASE == 1 ? 32 : 28;
  constexpr int KS = PHASE == 1 ? KS1 : KS2;
  constexpr int PITCH = PHASE == 1 ? F * 2 : S * 2;
  constexpr int PF = 4;
  __shared__ __attribute__((aligned(16))) char smem[49152];
  const int b = blockIdx.x, w = threadIdx.x >> 6, l = threadIdx.x & 63;
  const int x = b & 7, r = b >> 3;
  const int dt = 4 * (x & 1) + (r & 3);
  const int nt = (PHASE == 1 ? 7 : 8) * (x >> 1) + (r >> 2);
  if (PHASE == 1 && r >= 28) return;
  char* ring = smem + w * 4096;
  const int ks0 = w * NSTEP;
  const u32x4* ap = act + ((size_t)(dt * KS + ks0) * 2) * 64 + l;
  const int wrow = l >> 3, wch = l & 7;
  const char* wp = W + (size_t)(ks0 * 16 + wrow) * PITCH + nt * 128 + wch * 16;
  const int wst0 = wrow * 128 + 16 * (wch ^ (((wrow >> 1) & 1) << 2));
  const int hh = l >> 5, blk = (l >> 4) & 1, q = (l & 15) >> 2, p = l & 3;
  int tra[2][2];
#pragma unroll
  for (int nb = 0; nb < 2; ++nb)
#pragma unroll
    for (int t = 0; t < 2; ++t) {
      const int row = PHASE == 1 ? (8 * hh + 4 * t + q) : (8 * t + 4 * hh + q);
      const int ch = 4 * nb + 2 * blk + (p >> 1);
      tra[nb][t] = row * 128 + 16 * (ch ^ (((row >> 1) & 1) << 2)) + 8 * (p & 1);
    }
  f32x16 acc[2][2] = {};
  u32x4 wr[PF][2], ar[PF][2];
#pragma unroll
  for (int i = 0; i < PF; ++i) {
    wr[i][0] = *(const u32x4*)(wp + (size_t)(i * 16) * PITCH);
    wr[i][1] = *(const u32x4*)(wp + (size_t)(i * 16 + 8) * PITCH);
    ar[i][0] = ap[(i * 2) * 64];
    ar[i][1] = ap[(i * 2 + 1) * 64];
  }
#pragma unroll
  for (int i = 0; i < NSTEP; ++i) {
    char* slot = ring + (i & 1) * 2048;
    *(u32x4*)(slot + wst0) = wr[i % PF][0];
    *(u32x4*)(slot + wst0 + 1024) = wr[i % PF][1];
    u32x4 a0 = ar[i % PF][0], a1 = ar[i % PF][1];
    if (i + PF < NSTEP) {
      wr[i % PF][0] = *(const u32x4*)(wp + (size_t)((i + PF) * 16) * PITCH);
      wr[i % PF][1] = *(const u32x4*)(wp + (size_t)((i + PF) * 16 + 8) * PITCH);
      ar[i % PF][0] = ap[((i + PF) * 2) * 64];
      ar[i % PF][1] = ap[((i + PF) * 2 + 1) * 64];
    }
    bf16x8 wf[2];
#pragma unroll
    for (int nb = 0; nb < 2; ++nb) {
      s16x4 lo = __builtin_amdgcn_ds_read_tr16_b64_v4i16((LAS s16x4*)(slot + tra[nb][0]));
      s16x4 hi = __builtin_amdgcn_ds_read_tr16_b64_v4i16((LAS s16x4*)(slot + tra[nb][1]));
      u32x2 a = __builtin_bit_cast(u32x2, lo), c = __builtin_bit_cast(u32x2, hi);
      wf[nb] = __builtin_bit_cast(bf16x8, (u32x4){a[0], a[1], c[0], c[1]});
    }
    bf16x8 af[2] = {__builtin_bit_cast(bf16x8, a0), __builtin_bit_cast(bf16x8, a1)};
#pragma unroll
    for (int i0 = 0; i0 < 2; ++i0)
#pragma unroll
      for (int i1 = 0; i1 < 2; ++i1) {
        if (PHASE == 1) acc[i0][i1] = __builtin_amdgcn_mfma_f32_32x32x16_bf16(wf[i0], af[i1], acc[i0][i1], 0, 0, 0);
        else            acc[i0][i1] = __builtin_amdgcn_mfma_f32_32x32x16_bf16(af[i1], wf[i0], acc[i0][i1], 0, 0, 0);
      }
  }
  __syncthreads();
  f32x4* red = (f32x4*)smem;
#pragma unroll
  for (int dw = 0; dw < 4; ++dw) {
    if (dw != w) {
      const int slot = (w - dw - 1) & 3;
#pragma unroll
      for (int g = 0; g < 4; ++g) {
        const f32x16& a = acc[dw >> 1][dw & 1];
        red[((dw * 3 + slot) * 4 + g) * 64 + l] = (f32x4){a[4 * g], a[4 * g + 1], a[4 * g + 2], a[4 * g + 3]};
      }
    }
  }
  __syncthreads();
  f32x16 v = (w == 0) ? acc[0][0] : (w == 1) ? acc[0][1] : (w == 2) ? acc[1][0] : acc[1][1];
#pragma unroll
  for (int slot = 0; slot < 3; ++slot)
#pragma unroll
    for (int g = 0; g < 4; ++g) {
      f32x4 t = red[((w * 3 + slot) * 4 + g) * 64 + l];
#pragma unroll
      for (int e = 0; e < 4; ++e) v[4 * g + e] += t[e];
    }
  const int i0 = w >> 1, i1 = w & 1;
  if (PHASE == 1) {
    u32x4* GF = (u32x4*)outp;
    unsigned o[8];
#pragma unroll
    for (int g = 0; g < 4; ++g) {
      const int f = 64 * nt + 32 * i0 + 8 * g + 4 * hh;
      u32x2 bb = *(const u32x2*)(bias + f);
      float bv[4] = {bflo(bb[0]), bfhi(bb[0]), bflo(bb[1]), bfhi(bb[1])};
      float gl[4];
#pragma unroll
      for (int e = 0; e < 4; ++e) {
        float u = rbf(rbf(v[4 * g + e]) + bv[e]);
        float z = u * (-2.3022081978f - 0.1029432397f * u * u);
        gl[e] = u * __builtin_amdgcn_rcpf(1.0f + __builtin_amdgcn_exp2f(z));
      }
      o[2 * g] = cvtpk(gl[0], gl[1]); o[2 * g + 1] = cvtpk(gl[2], gl[3]);
    }
#pragma unroll
    for (int s2 = 0; s2 < 2; ++s2)
      GF[((dt * KS2 + 4 * nt + 2 * i0 + s2) * 2 + i1) * 64 + l] = (u32x4){o[4 * s2], o[4 * s2 + 1], o[4 * s2 + 2], o[4 * s2 + 3]};
  } else {
    unsigned short* out = (unsigned short*)outp;
    const int s = 64 * nt + 32 * i0 + (l & 31);
    const float bb = bflo(bias[s]);
#pragma unroll
    for (int g = 0; g < 4; ++g) {
      float y[4];
#pragma unroll
      for (int e = 0; e < 4; ++e) y[e] = rbf(v[4 * g + e]) + bb;
      *(u32x2*)(out + (size_t)s * D + 64 * dt + 32 * i1 + 8 * g + 4 * hh) = (u32x2){cvtpk(y[0], y[1]), cvtpk(y[2], y[3])};
    }
  }
}

extern "C" void kernel_launch(void* const* d_in, const int* in_sizes, int n_in, void* d_out, int out_size, void* d_ws, size_t ws_size,
                              hipStream_t stream) {
  const void* x = d_in[0]; const void* w1 = d_in[1]; const void* b1 = d_in[2]; const void* w2 = d_in[3]; const void* b2 = d_in[4];
  char* ws = (char*)d_ws;
  u32x4* hF = (u32x4*)ws;
  u32x4* GF = (u32x4*)(ws + (2u << 20));
  k_ln<<<256, 256, 0, stream>>>((const u32x4*)x, hF);
  k_gemm<1><<<256, 256, 0, stream>>>(hF, (const char*)w1, (const unsigned short*)b1, GF);
  k_gemm<2><<<256, 256, 0, stream>>>(GF, (const char*)w2, (const unsigned short*)b2, d_out);
}
```

```cpp
#include <hip/hip_runtime.h>
#include <stdint.h>

__global__ __launch_bounds__(256, 1) void standalone__mixer_token_mlp__prompt_kernel(const void* x, const void* w1, const void* b1, const void* w2, const void* b2,
                                                   void* out, void* ws, void* ws2) {
  __shared__ __attribute__((aligned(16))) char smem[163840];
  const unsigned lds = (unsigned)(size_t)(__attribute__((address_space(3))) char*)smem;
  asm volatile(
    "s_load_dwordx16 s[8:23], %0, 0x0\n"
    "s_mov_b32 s70, %1\n"
    "v_and_b32 v1, 63, v0\n"
    "v_lshrrev_b32 v22, 6, v0\n"
    "s_nop 0\n"
    "v_readfirstlane_b32 s54, v22\n"
    "s_getpc_b64 s[64:65]\n"
    "L_pc_%=:\n"
    "v_lshlrev_b32 v23, 7, v0\n"
    "v_min_u32 v23, L_end_%=-L_pc_%=, v23\n"
    "v_cmp_gt_u32 vcc, L_end_%=-L_pc_%=, v23\n"
    "s_nop 0\n"
    "s_and_saveexec_b64 s[66:67], vcc\n"
    "s_cbranch_execz L_nopf_%=\n"
    "global_load_dword v226, v23, s[64:65]\n"
    "L_nopf_%=:\n"
    "s_mov_b64 exec, -1\n"
    "s_and_b32 s66, s70, 7\n"
    "s_lshr_b32 s67, s70, 3\n"
    "s_and_b32 s91, s67, 7\n"
    "s_lshr_b32 s68, s67, 3\n"
    "s_lshl_b32 s57, s66, 2\n"
    "s_add_u32 s57, s57, s68\n"
    "s_lshr_b32 s56, s66, 1\n"
    "s_mul_i32 s56, s56, 7\n"
    "s_and_b32 s69, s66, 1\n"
    "s_lshl_b32 s69, s69, 2\n"
    "s_add_u32 s64, s56, s69\n"
    "s_add_u32 s64, s64, s68\n"
    "s_add_u32 s65, s56, 3\n"
    "s_and_b32 s55, s67, 3\n"
    "s_add_u32 s55, s55, s69\n"
    "s_cmp_lt_u32 s68, 3\n"
    "s_cselect_b32 s56, s64, s65\n"
    "s_cselect_b32 s55, s91, s55\n"
    "s_cmp_ge_u32 s67, 28\n"
    "s_cselect_b32 s58, 1, 0\n"
    "s_mul_i32 s69, s66, 1\n"
    "s_and_b32 s64, s55, 3\n"
    "s_lshl_b32 s64, s64, 1\n"
    "s_cmp_lt_u32 s68, 3\n"
    "s_cselect_b32 s64, s55, s64\n"
    "s_add_u32 s64, s64, s69\n"
    "s_and_b32 s81, s64, 7\n"
    "s_add_u32 s69, s69, s91\n"
    "s_mul_i32 s67, s69, 37\n"
    "s_lshr_b32 s67, s67, 8\n"
    "s_mul_i32 s67, s67, 7\n"
    "s_sub_u32 s82, s69, s67\n"
    "s_mul_i32 s88, s56, 37\n"
    "s_lshr_b32 s88, s88, 8\n"
    "s_mul_i32 s89, s88, 7\n"
    "s_sub_u32 s89, s56, s89\n"
    "s_and_b32 s90, s70, 7\n"
    "s_mul_i32 s62, s54, 28672\n"
    "v_lshlrev_b32 v12, 4, v1\n"
    "v_lshrrev_b32 v22, 3, v1\n"
    "v_lshlrev_b32 v23, 1, v22\n"
    "v_and_b32 v23, 4, v23\n"
    "v_and_b32 v24, 7, v1\n"
    "v_xor_b32 v23, v23, v24\n"
    "v_lshlrev_b32 v23, 4, v23\n"
    "v_lshrrev_b32 v24, 2, v22\n"
    "v_lshl_add_u32 v24, v24, 2, v22\n"
    "v_lshl_add_u32 v3, v24, 12, v23\n"
    "v_mul_u32_u24 v25, 3584, v22\n"
    "v_add_u32 v2, v25, v23\n"
    "s_mul_i32 s78, s54, 1835008\n"
    "s_lshl_b32 s67, s57, 7\n"
    "s_add_u32 s78, s78, s67\n"
    "s_mul_i32 s74, s54, 1835008\n"
    "s_lshl_b32 s67, s56, 7\n"
    "s_add_u32 s74, s74, s67\n"
    "s_mov_b32 s75, s81\n"
    "s_mov_b32 s79, s82\n"
    "s_lshl_b32 s66, s70, 3\n"
    "s_lshl_b32 s67, s54, 1\n"
    "s_add_u32 s66, s66, s67\n"
    "s_lshl_b32 s66, s66, 10\n"
    "s_waitcnt lgkmcnt(0)\n"
    "s_mov_b32 s24, s8\n"
    "s_and_b32 s25, s9, 0xffff\n"
    "s_mov_b32 s26, 0x200000\n"
    "s_mov_b32 s27, 0x20000\n"
    "buffer_load_dwordx4 v[240:243], v12, s[24:27], s66 offen \n"
    "buffer_load_dwordx4 v[244:247], v12, s[24:27], s66 offen offset:1024 \n"
    "s_mov_b32 s28, s10\n"
    "s_and_b32 s29, s11, 0xffff\n"
    "s_mov_b32 s30, 0x700000\n"
    "s_mov_b32 s31, 0x20000\n"
    "s_add_u32 s48, s20, 0x400000\n"
    "s_addc_u32 s49, s21, 0\n"
    "v_mov_b32 v30, 0\n"
    "global_load_dwordx2 v[20:21], v30, s[48:49] sc1\n"
    "s_mov_b32 s96, s14\n"
    "s_and_b32 s97, s15, 0xffff\n"
    "s_mov_b32 s98, 0x700000\n"
    "s_mov_b32 s99, 0x20000\n"
    "s_add_u32 s36, s20, 0x1000\n"
    "s_addc_u32 s37, s21, 0\n"
    "s_and_b32 s37, s37, 0xffff\n"
    "s_mov_b32 s38, 0x200000\n"
    "s_mov_b32 s39, 0x20000\n"
    "s_add_u32 s40, s20, 0x201000\n"
    "s_addc_u32 s41, s21, 0\n"
    "s_and_b32 s41, s41, 0xffff\n"
    "s_mov_b32 s42, 0x1c0000\n"
    "s_mov_b32 s43, 0x20000\n"
    "s_mov_b32 s44, s18\n"
    "s_and_b32 s45, s19, 0xffff\n"
    "s_mov_b32 s46, 0x200000\n"
    "s_mov_b32 s47, 0x20000\n"
    "s_lshl_b32 s66, s56, 7\n"
    "s_lshr_b32 s67, s54, 1\n"
    "s_lshl_b32 s67, s67, 6\n"
    "s_add_u32 s66, s66, s67\n"
    "s_min_u32 s68, s66, 3520\n"
    "s_add_u32 s60, s12, s68\n"
    "s_addc_u32 s61, s13, 0\n"
    "s_lshl_b32 s66, s57, 7\n"
    "s_add_u32 s66, s66, s67\n"
    "v_and_b32 v23, 31, v1\n"
    "v_lshlrev_b32 v23, 1, v23\n"
    "v_add_u32 v23, s66, v23\n"
    "global_load_ushort v216, v23, s[16:17]\n"
    "s_load_dwordx16 s[8:23], s[60:61], 0x0\n"
    "s_cmp_eq_u32 s58, 1\n"
    "s_cbranch_scc1 L_idle_%=\n"
    "s_add_u32 m0, s62, 0\n"
    "s_mul_i32 s76, s75, 229376\n"
    "s_add_u32 s76, s76, s74\n"
    "buffer_load_dwordx4 v2, s[28:31], s76 offen  lds\n"
    "s_add_u32 m0, s62, 1024\n"
    "s_add_u32 s76, s76, 28672\n"
    "buffer_load_dwordx4 v2, s[28:31], s76 offen  lds\n"
    "s_add_u32 m0, s62, 2048\n"
    "s_add_u32 s76, s76, 28672\n"
    "buffer_load_dwordx4 v2, s[28:31], s76 offen  lds\n"
    "s_add_u32 m0, s62, 3072\n"
    "s_add_u32 s76, s76, 28672\n"
    "buffer_load_dwordx4 v2, s[28:31], s76 offen  lds\n"
    "s_add_u32 m0, s62, 4096\n"
    "s_add_u32 s76, s76, 28672\n"
    "buffer_load_dwordx4 v2, s[28:31], s76 offen  lds\n"
    "s_add_u32 m0, s62, 5120\n"
    "s_add_u32 s76, s76, 28672\n"
    "buffer_load_dwordx4 v2, s[28:31], s76 offen  lds\n"
    "s_add_u32 m0, s62, 6144\n"
    "s_add_u32 s76, s76, 28672\n"
    "buffer_load_dwordx4 v2, s[28:31], s76 offen  lds\n"
    "s_add_u32 m0, s62, 7168\n"
    "s_add_u32 s76, s76, 28672\n"
    "buffer_load_dwordx4 v2, s[28:31], s76 offen  lds\n"
    "s_add_u32 m0, s62, 8192\n"
    "s_add_u32 s75, s75, 1\n"
    "s_cmp_eq_u32 s75, 8\n"
    "s_cselect_b32 s75, 0, s75\n"
    "s_mul_i32 s76, s75, 229376\n"
    "s_add_u32 s76, s76, s74\n"
    "buffer_load_dwordx4 v2, s[28:31], s76 offen  lds\n"
    "s_add_u32 m0, s62, 9216\n"
    "s_add_u32 s76, s76, 28672\n"
    "buffer_load_dwordx4 v2, s[28:31], s76 offen  lds\n"
    "v_lshrrev_b32 v22, 5, v1\n"
    "v_bfe_u32 v23, v1, 4, 1\n"
    "v_bfe_u32 v24, v1, 2, 2\n"
    "v_and_b32 v25, 3, v1\n"
    "v_lshl_add_u32 v26, v22, 3, v24\n"
    "v_lshlrev_b32 v26, 7, v26\n"
    "v_lshrrev_b32 v27, 1, v25\n"
    "v_lshl_add_u32 v27, v23, 1, v27\n"
    "v_lshrrev_b32 v28, 1, v24\n"
    "s_lshr_b32 s66, s54, 1\n"
    "v_xor_b32 v28, s66, v28\n"
    "v_lshl_add_u32 v27, v28, 2, v27\n"
    "v_lshl_add_u32 v26, v27, 4, v26\n"
    "v_and_b32 v27, 1, v25\n"
    "v_lshl_add_u32 v26, v27, 3, v26\n"
    "v_add_u32 v4, s62, v26\n"
    "v_xor_b32 v5, 64, v4\n"
    "v_mul_u32_u24 v26, 1088, v24\n"
    "v_lshlrev_b32 v27, 6, v22\n"
    "v_lshl_add_u32 v27, v23, 5, v27\n"
    "v_lshl_add_u32 v27, v25, 3, v27\n"
    "v_add_u32 v26, v26, v27\n"
    "s_lshl_b32 s67, s54, 8\n"
    "s_add_u32 s67, s67, 114688\n"
    "v_add_u32 v17, s67, v26\n"
    "s_mul_i32 s67, s54, 2176\n"
    "s_add_u32 s67, s67, 114688\n"
    "v_add_u32 v16, s67, v12\n"
    "v_and_b32 v26, 31, v1\n"
    "v_lshlrev_b32 v26, 4, v26\n"
    "v_lshl_add_u32 v18, v22, 10, v26\n"
    "v_and_b32 v26, 31, v1\n"
    "v_lshlrev_b32 v26, 10, v26\n"
    "v_lshl_add_u32 v13, v22, 4, v26\n"
    "s_and_b32 s67, s54, 1\n"
    "s_lshl_b32 s67, s67, 10\n"
    "v_add_u32 v6, s67, v12\n"
    "v_xor_b32 v7, 0x400, v6\n"
    "s_xor_b32 s67, s54, 1\n"
    "s_mul_i32 s67, s67, 12288\n"
    "s_add_u32 s67, s67, 114688\n"
    "v_add_u32 v8, s67, v12\n"
    "s_xor_b32 s67, s54, 2\n"
    "s_mul_i32 s67, s67, 12288\n"
    "s_add_u32 s67, s67, 118784\n"
    "v_add_u32 v9, s67, v12\n"
    "s_xor_b32 s67, s54, 3\n"
    "s_mul_i32 s67, s67, 12288\n"
    "s_add_u32 s67, s67, 122880\n"
    "v_add_u32 v10, s67, v12\n"
    "s_mul_i32 s67, s54, 12288\n"
    "s_add_u32 s67, s67, 114688\n"
    "v_add_u32 v11, s67, v12\n"
    "s_and_b32 s68, s70, 7\n"
    "s_lshl_b32 s68, s68, 11\n"
    "s_lshl_b32 s67, s54, 9\n"
    "s_add_u32 s67, s67, s68\n"
    "s_add_u32 s67, s67, 4096\n"
    "v_lshlrev_b32 v26, 3, v1\n"
    "v_add_u32 v14, s67, v26\n"
    "s_lshl_b32 s68, s90, 5\n"
    "s_lshl_b32 s67, s91, 2\n"
    "s_add_u32 s67, s67, s68\n"
    "s_add_u32 s67, s67, s54\n"
    "s_lshl_b32 s67, s67, 7\n"
    "s_add_u32 s67, s67, 20480\n"
    "v_min_u32 v26, 6, v1\n"
    "v_lshlrev_b32 v26, 3, v26\n"
    "v_add_u32 v15, s67, v26\n"
    "v_mov_b32 v218, 0xc0135761\n"
    "v_mov_b32 v219, 0xc0135761\n"
    "v_mov_b32 v220, 0xbdd2d3e8\n"
    "v_mov_b32 v221, 0xbdd2d3e8\n"
    "v_mov_b32 v222, 1.0\n"
    "v_mov_b32 v223, 1.0\n"
    "s_waitcnt vmcnt(12)\n"
    "s_mov_b32 s69, 0x3b000000\n"
    "v_lshlrev_b32 v96, 16, v240\n"
    "v_and_b32 v97, 0xffff0000, v240\n"
    "v_lshlrev_b32 v98, 16, v241\n"
    "v_and_b32 v99, 0xffff0000, v241\n"
    "v_lshlrev_b32 v100, 16, v242\n"
    "v_and_b32 v101, 0xffff0000, v242\n"
    "v_lshlrev_b32 v102, 16, v243\n"
    "v_and_b32 v103, 0xffff0000, v243\n"
    "v_lshlrev_b32 v104, 16, v244\n"
    "v_and_b32 v105, 0xffff0000, v244\n"
    "v_lshlrev_b32 v106, 16, v245\n"
    "v_and_b32 v107, 0xffff0000, v245\n"
    "v_lshlrev_b32 v108, 16, v246\n"
    "v_and_b32 v109, 0xffff0000, v246\n"
    "v_lshlrev_b32 v110, 16, v247\n"
    "v_and_b32 v111, 0xffff0000, v247\n"
    "v_pk_add_f32 v[112:113], v[96:97], v[98:99]\n"
    "v_pk_add_f32 v[114:115], v[100:101], v[102:103]\n"
    "v_pk_add_f32 v[116:117], v[104:105], v[106:107]\n"
    "v_pk_add_f32 v[118:119], v[108:109], v[110:111]\n"
    "v_pk_add_f32 v[112:113], v[112:113], v[114:115]\n"
    "v_pk_add_f32 v[116:117], v[116:117], v[118:119]\n"
    "v_add_f32 v112, v112, v113\n"
    "v_add_f32 v116, v116, v117\n"
    "s_nop 0\n"
    "v_add_f32_dpp v112, v112, v112 row_shr:1 row_mask:0xf bank_mask:0xf\n"
    "v_add_f32_dpp v116, v116, v116 row_shr:1 row_mask:0xf bank_mask:0xf\n"
    "s_nop 0\n"
    "v_add_f32_dpp v112, v112, v112 row_shr:2 row_mask:0xf bank_mask:0xf\n"
    "v_add_f32_dpp v116, v116, v116 row_shr:2 row_mask:0xf bank_mask:0xf\n"
    "s_nop 0\n"
    "v_add_f32_dpp v112, v112, v112 row_shr:4 row_mask:0xf bank_mask:0xf\n"
    "v_add_f32_dpp v116, v116, v116 row_shr:4 row_mask:0xf bank_mask:0xf\n"
    "s_nop 0\n"
    "v_add_f32_dpp v112, v112, v112 row_shr:8 row_mask:0xf bank_mask:0xf\n"
    "v_add_f32_dpp v116, v116, v116 row_shr:8 row_mask:0xf bank_mask:0xf\n"
    "s_nop 0\n"
    "v_add_f32_dpp v112, v112, v112 row_bcast:15 row_mask:0xa bank_mask:0xf\n"
    "v_add_f32_dpp v116, v116, v116 row_bcast:15 row_mask:0xa bank_mask:0xf\n"
    "s_nop 0\n"
    "v_add_f32_dpp v112, v112, v112 row_bcast:31 row_mask:0xc bank_mask:0xf\n"
    "v_add_f32_dpp v116, v116, v116 row_bcast:31 row_mask:0xc bank_mask:0xf\n"
    "v_mul_f32 v112, 0x3b000000, v112\n"
    "v_mul_f32 v116, 0x3b000000, v116\n"
    "v_cvt_pk_bf16_f32 v120, v112, v112\n"
    "v_cvt_pk_bf16_f32 v121, v116, v116\n"
    "v_and_b32 v120, 0xffff0000, v120\n"
    "v_and_b32 v121, 0xffff0000, v121\n"
    "v_readlane_b32 s64, v120, 63\n"
    "v_readlane_b32 s66, v121, 63\n"
    "v_sub_f32 v112, v112, v120\n"
    "v_sub_f32 v116, v116, v121\n"
    "v_pk_add_f32 v[96:97], v[96:97], s[64:65] op_sel_hi:[1,0] neg_lo:[0,1] neg_hi:[0,1]\n"
    "v_pk_add_f32 v[98:99], v[98:99], s[64:65] op_sel_hi:[1,0] neg_lo:[0,1] neg_hi:[0,1]\n"
    "v_pk_add_f32 v[100:101], v[100:101], s[64:65] op_sel_hi:[1,0] neg_lo:[0,1] neg_hi:[0,1]\n"
    "v_pk_add_f32 v[102:103], v[102:103], s[64:65] op_sel_hi:[1,0] neg_lo:[0,1] neg_hi:[0,1]\n"
    "v_pk_add_f32 v[104:105], v[104:105], s[66:67] op_sel_hi:[1,0] neg_lo:[0,1] neg_hi:[0,1]\n"
    "v_pk_add_f32 v[106:107], v[106:107], s[66:67] op_sel_hi:[1,0] neg_lo:[0,1] neg_hi:[0,1]\n"
    "v_pk_add_f32 v[108:109], v[108:109], s[66:67] op_sel_hi:[1,0] neg_lo:[0,1] neg_hi:[0,1]\n"
    "v_pk_add_f32 v[110:111], v[110:111], s[66:67] op_sel_hi:[1,0] neg_lo:[0,1] neg_hi:[0,1]\n"
    "v_pk_mul_f32 v[122:123], v[96:97], v[96:97]\n"
    "v_pk_mul_f32 v[124:125], v[104:105], v[104:105]\n"
    "v_pk_fma_f32 v[122:123], v[98:99], v[98:99], v[122:123]\n"
    "v_pk_fma_f32 v[124:125], v[106:107], v[106:107], v[124:125]\n"
    "v_pk_fma_f32 v[122:123], v[100:101], v[100:101], v[122:123]\n"
    "v_pk_fma_f32 v[124:125], v[108:109], v[108:109], v[124:125]\n"
    "v_pk_fma_f32 v[122:123], v[102:103], v[102:103], v[122:123]\n"
    "v_pk_fma_f32 v[124:125], v[110:111], v[110:111], v[124:125]\n"
    "v_add_f32 v122, v122, v123\n"
    "v_add_f32 v124, v124, v125\n"
    "s_nop 0\n"
    "v_add_f32_dpp v122, v122, v122 row_shr:1 row_mask:0xf bank_mask:0xf\n"
    "v_add_f32_dpp v124, v124, v124 row_shr:1 row_mask:0xf bank_mask:0xf\n"
    "s_nop 0\n"
    "v_add_f32_dpp v122, v122, v122 row_shr:2 row_mask:0xf bank_mask:0xf\n"
    "v_add_f32_dpp v124, v124, v124 row_shr:2 row_mask:0xf bank_mask:0xf\n"
    "s_nop 0\n"
    "v_add_f32_dpp v122, v122, v122 row_shr:4 row_mask:0xf bank_mask:0xf\n"
    "v_add_f32_dpp v124, v124, v124 row_shr:4 row_mask:0xf bank_mask:0xf\n"
    "s_nop 0\n"
    "v_add_f32_dpp v122, v122, v122 row_shr:8 row_mask:0xf bank_mask:0xf\n"
    "v_add_f32_dpp v124, v124, v124 row_shr:8 row_mask:0xf bank_mask:0xf\n"
    "s_nop 0\n"
    "v_add_f32_dpp v122, v122, v122 row_bcast:15 row_mask:0xa bank_mask:0xf\n"
    "v_add_f32_dpp v124, v124, v124 row_bcast:15 row_mask:0xa bank_mask:0xf\n"
    "s_nop 0\n"
    "v_add_f32_dpp v122, v122, v122 row_bcast:31 row_mask:0xc bank_mask:0xf\n"
    "v_add_f32_dpp v124, v124, v124 row_bcast:31 row_mask:0xc bank_mask:0xf\n"
    "v_mul_f32 v112, v112, v112\n"
    "v_mul_f32 v116, v116, v116\n"
    "v_fma_f32 v122, v122, s69, -v112\n"
    "v_fma_f32 v124, v124, s69, -v116\n"
    "v_max_f32 v122, 0, v122\n"
    "v_max_f32 v124, 0, v124\n"
    "v_cvt_pk_bf16_f32 v122, v122, v122\n"
    "v_cvt_pk_bf16_f32 v124, v124, v124\n"
    "v_and_b32 v122, 0xffff0000, v122\n"
    "v_and_b32 v124, 0xffff0000, v124\n"
    "v_add_f32 v122, 0x3727c5ac, v122\n"
    "v_add_f32 v124, 0x3727c5ac, v124\n"
    "v_cvt_pk_bf16_f32 v122, v122, v122\n"
    "v_cvt_pk_bf16_f32 v124, v124, v124\n"
    "v_and_b32 v122, 0xffff0000, v122\n"
    "v_and_b32 v124, 0xffff0000, v124\n"
    "v_sqrt_f32 v122, v122\n"
    "v_sqrt_f32 v124, v124\n"
    "v_cvt_pk_bf16_f32 v122, v122, v122\n"
    "v_cvt_pk_bf16_f32 v124, v124, v124\n"
    "v_and_b32 v122, 0xffff0000, v122\n"
    "v_and_b32 v124, 0xffff0000, v124\n"
    "v_rcp_f32 v122, v122\n"
    "v_rcp_f32 v124, v124\n"
    "v_readlane_b32 s64, v122, 63\n"
    "v_readlane_b32 s66, v124, 63\n"
    "s_nop 0\n"
    "v_pk_mul_f32 v[96:97], v[96:97], s[64:65] op_sel_hi:[1,0]\n"
    "v_pk_mul_f32 v[98:99], v[98:99], s[64:65] op_sel_hi:[1,0]\n"
    "v_pk_mul_f32 v[100:101], v[100:101], s[64:65] op_sel_hi:[1,0]\n"
    "v_pk_mul_f32 v[102:103], v[102:103], s[64:65] op_sel_hi:[1,0]\n"
    "v_pk_mul_f32 v[104:105], v[104:105], s[66:67] op_sel_hi:[1,0]\n"
    "v_pk_mul_f32 v[106:107], v[106:107], s[66:67] op_sel_hi:[1,0]\n"
    "v_pk_mul_f32 v[108:109], v[108:109], s[66:67] op_sel_hi:[1,0]\n"
    "v_pk_mul_f32 v[110:111], v[110:111], s[66:67] op_sel_hi:[1,0]\n"
    "v_cvt_pk_bf16_f32 v128, v96, v97\n"
    "v_cvt_pk_bf16_f32 v129, v98, v99\n"
    "v_cvt_pk_bf16_f32 v130, v100, v101\n"
    "v_cvt_pk_bf16_f32 v131, v102, v103\n"
    "v_cvt_pk_bf16_f32 v132, v104, v105\n"
    "v_cvt_pk_bf16_f32 v133, v106, v107\n"
    "v_cvt_pk_bf16_f32 v134, v108, v109\n"
    "v_cvt_pk_bf16_f32 v135, v110, v111\n"
    "ds_write_b128 v16, v[128:131]\n"
    "ds_write_b128 v16, v[132:135] offset:1088\n"
    "s_lshl_b32 s64, s54, 19\n"
    "s_lshr_b32 s65, s70, 1\n"
    "s_lshl_b32 s65, s65, 11\n"
    "s_add_u32 s64, s64, s65\n"
    "s_and_b32 s65, s70, 1\n"
    "s_lshl_b32 s65, s65, 9\n"
    "s_add_u32 s64, s64, s65\n"
    "s_add_u32 s65, s64, 0x40000\n"
    "s_waitcnt lgkmcnt(0)\n"
    "s_barrier\n"
    "ds_read_b64_tr_b16 v[136:137], v17 offset:0\n"
    "ds_read_b64_tr_b16 v[138:139], v17 offset:4352\n"
    "ds_read_b64_tr_b16 v[140:141], v17 offset:128\n"
    "ds_read_b64_tr_b16 v[142:143], v17 offset:4480\n"
    "s_waitcnt vmcnt(11)\n"
    "v_readfirstlane_b32 s52, v20\n"
    "v_readfirstlane_b32 s53, v21\n"
    "s_add_u32 s52, s52, 1\n"
    "s_addc_u32 s53, s53, 0\n"
    "s_mul_i32 s50, s52, 0x7f4a7c15\n"
    "s_mul_hi_u32 s51, s52, 0x7f4a7c15\n"
    "s_mul_i32 s66, s52, 0x9e3779b9\n"
    "s_add_u32 s51, s51, s66\n"
    "s_mul_i32 s66, s53, 0x7f4a7c15\n"
    "s_add_u32 s51, s51, s66\n"
    "v_mov_b32 v20, s50\n"
    "v_mov_b32 v21, s51\n"
    "s_waitcnt lgkmcnt(0)\n"
    "buffer_store_dwordx4 v[136:139], v18, s[36:39], s64 offen sc1\n"
    "buffer_store_dwordx4 v[140:143], v18, s[36:39], s65 offen sc1\n"
    "s_add_u32 m0, s62, 10240\n"
    "s_add_u32 s76, s76, 28672\n"
    "buffer_load_dwordx4 v2, s[28:31], s76 offen  lds\n"
    "s_add_u32 m0, s62, 11264\n"
    "s_add_u32 s76, s76, 28672\n"
    "buffer_load_dwordx4 v2, s[28:31], s76 offen  lds\n"
    "s_add_u32 m0, s62, 12288\n"
    "s_add_u32 s76, s76, 28672\n"
    "buffer_load_dwordx4 v2, s[28:31], s76 offen  lds\n"
    "s_add_u32 m0, s62, 13312\n"
    "s_add_u32 s76, s76, 28672\n"
    "buffer_load_dwordx4 v2, s[28:31], s76 offen  lds\n"
    "s_add_u32 m0, s62, 14336\n"
    "s_add_u32 s76, s76, 28672\n"
    "buffer_load_dwordx4 v2, s[28:31], s76 offen  lds\n"
    "s_add_u32 m0, s62, 15360\n"
    "s_add_u32 s76, s76, 28672\n"
    "buffer_load_dwordx4 v2, s[28:31], s76 offen  lds\n"
    "s_waitcnt vmcnt(6)\n"
    "s_barrier\n"
    "s_lshl_b32 s64, s70, 3\n"
    "s_lshl_b32 s65, s54, 12\n"
    "s_add_u32 s64, s64, s65\n"
    "s_add_u32 s64, s64, 4096\n"
    "s_cmp_eq_u32 s54, 0\n"
    "s_cbranch_scc0 L_nf1_%=\n"
    "v_lshlrev_b32 v22, 11, v1\n"
    "v_add_u32 v22, s64, v22\n"
    "s_mov_b64 exec, 255\n"
    "global_store_dwordx2 v22, v[20:21], s[48:49] sc1\n"
    "s_mov_b64 exec, -1\n"
    "L_nf1_%=:\n"
    "s_add_u32 m0, s62, 16384\n"
    "s_add_u32 s75, s75, 1\n"
    "s_cmp_eq_u32 s75, 8\n"
    "s_cselect_b32 s75, 0, s75\n"
    "s_mul_i32 s76, s75, 229376\n"
    "s_add_u32 s76, s76, s74\n"
    "buffer_load_dwordx4 v2, s[28:31], s76 offen  lds\n"
    "s_add_u32 m0, s62, 17408\n"
    "s_add_u32 s76, s76, 28672\n"
    "buffer_load_dwordx4 v2, s[28:31], s76 offen  lds\n"
    "s_add_u32 m0, s62, 18432\n"
    "s_add_u32 s76, s76, 28672\n"
    "buffer_load_dwordx4 v2, s[28:31], s76 offen  lds\n"
    "s_add_u32 m0, s62, 19456\n"
    "s_add_u32 s76, s76, 28672\n"
    "buffer_load_dwordx4 v2, s[28:31], s76 offen  lds\n"
    "s_add_u32 m0, s62, 20480\n"
    "s_add_u32 s76, s76, 28672\n"
    "buffer_load_dwordx4 v2, s[28:31], s76 offen  lds\n"
    "s_add_u32 m0, s62, 21504\n"
    "s_add_u32 s76, s76, 28672\n"
    "buffer_load_dwordx4 v2, s[28:31], s76 offen  lds\n"
    "s_add_u32 m0, s62, 22528\n"
    "s_add_u32 s76, s76, 28672\n"
    "buffer_load_dwordx4 v2, s[28:31], s76 offen  lds\n"
    "s_add_u32 m0, s62, 23552\n"
    "s_add_u32 s76, s76, 28672\n"
    "buffer_load_dwordx4 v2, s[28:31], s76 offen  lds\n"
    "s_add_u32 m0, s62, 24576\n"
    "s_add_u32 s75, s75, 1\n"
    "s_cmp_eq_u32 s75, 8\n"
    "s_cselect_b32 s75, 0, s75\n"
    "s_mul_i32 s76, s75, 229376\n"
    "s_add_u32 s76, s76, s74\n"
    "buffer_load_dwordx4 v2, s[28:31], s76 offen  lds\n"
    "s_add_u32 m0, s62, 25600\n"
    "s_add_u32 s76, s76, 28672\n"
    "buffer_load_dwordx4 v2, s[28:31], s76 offen  lds\n"
    "s_add_u32 m0, s62, 26624\n"
    "s_add_u32 s76, s76, 28672\n"
    "buffer_load_dwordx4 v2, s[28:31], s76 offen  lds\n"
    "s_add_u32 m0, s62, 27648\n"
    "s_add_u32 s76, s76, 28672\n"
    "buffer_load_dwordx4 v2, s[28:31], s76 offen  lds\n"
    "s_waitcnt lgkmcnt(0)\n"
    "s_mov_b32 exec_lo, -1\n"
    "s_mov_b32 exec_hi, 0\n"
    "v_lshlrev_b32 v32, 16, s8\n"
    "v_mov_b32 v33, s8\n"
    "v_lshlrev_b32 v34, 16, s9\n"
    "v_mov_b32 v35, s9\n"
    "v_lshlrev_b32 v36, 16, s12\n"
    "v_mov_b32 v37, s12\n"
    "v_lshlrev_b32 v38, 16, s13\n"
    "v_mov_b32 v39, s13\n"
    "v_lshlrev_b32 v40, 16, s16\n"
    "v_mov_b32 v41, s16\n"
    "v_lshlrev_b32 v42, 16, s17\n"
    "v_mov_b32 v43, s17\n"
    "v_lshlrev_b32 v44, 16, s20\n"
    "v_mov_b32 v45, s20\n"
    "v_lshlrev_b32 v46, 16, s21\n"
    "v_mov_b32 v47, s21\n"
    "s_mov_b32 exec_lo, 0\n"
    "s_mov_b32 exec_hi, -1\n"
    "v_lshlrev_b32 v32, 16, s10\n"
    "v_mov_b32 v33, s10\n"
    "v_lshlrev_b32 v34, 16, s11\n"
    "v_mov_b32 v35, s11\n"
    "v_lshlrev_b32 v36, 16, s14\n"
    "v_mov_b32 v37, s14\n"
    "v_lshlrev_b32 v38, 16, s15\n"
    "v_mov_b32 v39, s15\n"
    "v_lshlrev_b32 v40, 16, s18\n"
    "v_mov_b32 v41, s18\n"
    "v_lshlrev_b32 v42, 16, s19\n"
    "v_mov_b32 v43, s19\n"
    "v_lshlrev_b32 v44, 16, s22\n"
    "v_mov_b32 v45, s22\n"
    "v_lshlrev_b32 v46, 16, s23\n"
    "v_mov_b32 v47, s23\n"
    "s_mov_b64 exec, -1\n"
    "v_and_b32 v33, 0xffff0000, v33\n"
    "v_and_b32 v35, 0xffff0000, v35\n"
    "v_and_b32 v37, 0xffff0000, v37\n"
    "v_and_b32 v39, 0xffff0000, v39\n"
    "v_and_b32 v41, 0xffff0000, v41\n"
    "v_and_b32 v43, 0xffff0000, v43\n"
    "v_and_b32 v45, 0xffff0000, v45\n"
    "v_and_b32 v47, 0xffff0000, v47\n"
    "ds_read_b64_tr_b16 a[0:1], v4 offset:0\n"
    "ds_read_b64_tr_b16 a[2:3], v4 offset:512\n"
    "ds_read_b64_tr_b16 a[4:5], v5 offset:0\n"
    "ds_read_b64_tr_b16 a[6:7], v5 offset:512\n"
    "ds_read_b64_tr_b16 a[8:9], v4 offset:2048\n"
    "ds_read_b64_tr_b16 a[10:11], v4 offset:2560\n"
    "ds_read_b64_tr_b16 a[12:13], v5 offset:2048\n"
    "ds_read_b64_tr_b16 a[14:15], v5 offset:2560\n"
    "s_waitcnt lgkmcnt(0)\n"
    "s_add_u32 m0, s62, 0\n"
    "s_add_u32 s76, s76, 28672\n"
    "buffer_load_dwordx4 v2, s[28:31], s76 offen  lds\n"
    "s_add_u32 m0, s62, 1024\n"
    "s_add_u32 s76, s76, 28672\n"
    "buffer_load_dwordx4 v2, s[28:31], s76 offen  lds\n"
    "s_add_u32 m0, s62, 2048\n"
    "s_add_u32 s76, s76, 28672\n"
    "buffer_load_dwordx4 v2, s[28:31], s76 offen  lds\n"
    "s_add_u32 m0, s62, 3072\n"
    "s_add_u32 s76, s76, 28672\n"
    "buffer_load_dwordx4 v2, s[28:31], s76 offen  lds\n"
    "ds_read_b64_tr_b16 a[16:17], v4 offset:4096\n"
    "ds_read_b64_tr_b16 a[18:19], v4 offset:4608\n"
    "ds_read_b64_tr_b16 a[20:21], v5 offset:4096\n"
    "ds_read_b64_tr_b16 a[22:23], v5 offset:4608\n"
    "ds_read_b64_tr_b16 a[24:25], v4 offset:6144\n"
    "ds_read_b64_tr_b16 a[26:27], v4 offset:6656\n"
    "ds_read_b64_tr_b16 a[28:29], v5 offset:6144\n"
    "ds_read_b64_tr_b16 a[30:31], v5 offset:6656\n"
    "s_waitcnt lgkmcnt(0)\n"
    "s_add_u32 m0, s62, 4096\n"
    "s_add_u32 s75, s75, 1\n"
    "s_cmp_eq_u32 s75, 8\n"
    "s_cselect_b32 s75, 0, s75\n"
    "s_mul_i32 s76, s75, 229376\n"
    "s_add_u32 s76, s76, s74\n"
    "buffer_load_dwordx4 v2, s[28:31], s76 offen  lds\n"
    "s_add_u32 m0, s62, 5120\n"
    "s_add_u32 s76, s76, 28672\n"
    "buffer_load_dwordx4 v2, s[28:31], s76 offen  lds\n"
    "s_add_u32 m0, s62, 6144\n"
    "s_add_u32 s76, s76, 28672\n"
    "buffer_load_dwordx4 v2, s[28:31], s76 offen  lds\n"
    "s_add_u32 m0, s62, 7168\n"
    "s_add_u32 s76, s76, 28672\n"
    "buffer_load_dwordx4 v2, s[28:31], s76 offen  lds\n"
    "ds_read_b64_tr_b16 a[32:33], v4 offset:8192\n"
    "ds_read_b64_tr_b16 a[34:35], v4 offset:8704\n"
    "ds_read_b64_tr_b16 a[36:37], v5 offset:8192\n"
    "ds_read_b64_tr_b16 a[38:39], v5 offset:8704\n"
    "s_waitcnt vmcnt(24)\n"
    "ds_read_b64_tr_b16 a[40:41], v4 offset:10240\n"
    "ds_read_b64_tr_b16 a[42:43], v4 offset:10752\n"
    "ds_read_b64_tr_b16 a[44:45], v5 offset:10240\n"
    "ds_read_b64_tr_b16 a[46:47], v5 offset:10752\n"
    "s_waitcnt lgkmcnt(0)\n"
    "s_add_u32 m0, s62, 8192\n"
    "s_add_u32 s76, s76, 28672\n"
    "buffer_load_dwordx4 v2, s[28:31], s76 offen  lds\n"
    "s_add_u32 m0, s62, 9216\n"
    "s_add_u32 s76, s76, 28672\n"
    "buffer_load_dwordx4 v2, s[28:31], s76 offen  lds\n"
    "s_add_u32 m0, s62, 10240\n"
    "s_add_u32 s76, s76, 28672\n"
    "buffer_load_dwordx4 v2, s[28:31], s76 offen  lds\n"
    "s_add_u32 m0, s62, 11264\n"
    "s_add_u32 s76, s76, 28672\n"
    "buffer_load_dwordx4 v2, s[28:31], s76 offen  lds\n"
    "s_waitcnt vmcnt(26)\n"
    "ds_read_b64_tr_b16 a[48:49], v4 offset:12288\n"
    "ds_read_b64_tr_b16 a[50:51], v4 offset:12800\n"
    "ds_read_b64_tr_b16 a[52:53], v5 offset:12288\n"
    "ds_read_b64_tr_b16 a[54:55], v5 offset:12800\n"
    "s_waitcnt vmcnt(24)\n"
    "ds_read_b64_tr_b16 a[56:57], v4 offset:14336\n"
    "ds_read_b64_tr_b16 a[58:59], v4 offset:14848\n"
    "ds_read_b64_tr_b16 a[60:61], v5 offset:14336\n"
    "ds_read_b64_tr_b16 a[62:63], v5 offset:14848\n"
    "s_waitcnt lgkmcnt(0)\n"
    "s_add_u32 m0, s62, 12288\n"
    "s_add_u32 s75, s75, 1\n"
    "s_cmp_eq_u32 s75, 8\n"
    "s_cselect_b32 s75, 0, s75\n"
    "s_mul_i32 s76, s75, 229376\n"
    "s_add_u32 s76, s76, s74\n"
    "buffer_load_dwordx4 v2, s[28:31], s76 offen  lds\n"
    "s_add_u32 m0, s62, 13312\n"
    "s_add_u32 s76, s76, 28672\n"
    "buffer_load_dwordx4 v2, s[28:31], s76 offen  lds\n"
    "s_add_u32 m0, s62, 14336\n"
    "s_add_u32 s76, s76, 28672\n"
    "buffer_load_dwordx4 v2, s[28:31], s76 offen  lds\n"
    "s_add_u32 m0, s62, 15360\n"
    "s_add_u32 s76, s76, 28672\n"
    "buffer_load_dwordx4 v2, s[28:31], s76 offen  lds\n"
    "s_waitcnt vmcnt(26)\n"
    "ds_read_b64_tr_b16 a[64:65], v4 offset:16384\n"
    "ds_read_b64_tr_b16 a[66:67], v4 offset:16896\n"
    "ds_read_b64_tr_b16 a[68:69], v5 offset:16384\n"
    "ds_read_b64_tr_b16 a[70:71], v5 offset:16896\n"
    "s_waitcnt vmcnt(24)\n"
    "ds_read_b64_tr_b16 a[72:73], v4 offset:18432\n"
    "ds_read_b64_tr_b16 a[74:75], v4 offset:18944\n"
    "ds_read_b64_tr_b16 a[76:77], v5 offset:18432\n"
    "ds_read_b64_tr_b16 a[78:79], v5 offset:18944\n"
    "s_waitcnt lgkmcnt(0)\n"
    "s_add_u32 m0, s62, 16384\n"
    "s_add_u32 s76, s76, 28672\n"
    "buffer_load_dwordx4 v2, s[28:31], s76 offen  lds\n"
    "s_add_u32 m0, s62, 17408\n"
    "s_add_u32 s76, s76, 28672\n"
    "buffer_load_dwordx4 v2, s[28:31], s76 offen  lds\n"
    "s_add_u32 m0, s62, 18432\n"
    "s_add_u32 s76, s76, 28672\n"
    "buffer_load_dwordx4 v2, s[28:31], s76 offen  lds\n"
    "s_add_u32 m0, s62, 19456\n"
    "s_add_u32 s76, s76, 28672\n"
    "buffer_load_dwordx4 v2, s[28:31], s76 offen  lds\n"
    "s_waitcnt vmcnt(26)\n"
    "ds_read_b64_tr_b16 a[80:81], v4 offset:20480\n"
    "ds_read_b64_tr_b16 a[82:83], v4 offset:20992\n"
    "ds_read_b64_tr_b16 a[84:85], v5 offset:20480\n"
    "ds_read_b64_tr_b16 a[86:87], v5 offset:20992\n"
    "s_waitcnt vmcnt(24)\n"
    "ds_read_b64_tr_b16 a[88:89], v4 offset:22528\n"
    "ds_read_b64_tr_b16 a[90:91], v4 offset:23040\n"
    "ds_read_b64_tr_b16 a[92:93], v5 offset:22528\n"
    "ds_read_b64_tr_b16 a[94:95], v5 offset:23040\n"
    "s_waitcnt lgkmcnt(0)\n"
    "s_add_u32 m0, s62, 20480\n"
    "s_add_u32 s75, s75, 1\n"
    "s_cmp_eq_u32 s75, 8\n"
    "s_cselect_b32 s75, 0, s75\n"
    "s_mul_i32 s76, s75, 229376\n"
    "s_add_u32 s76, s76, s74\n"
    "buffer_load_dwordx4 v2, s[28:31], s76 offen  lds\n"
    "s_add_u32 m0, s62, 21504\n"
    "s_add_u32 s76, s76, 28672\n"
    "buffer_load_dwordx4 v2, s[28:31], s76 offen  lds\n"
    "s_add_u32 m0, s62, 22528\n"
    "s_add_u32 s76, s76, 28672\n"
    "buffer_load_dwordx4 v2, s[28:31], s76 offen  lds\n"
    "s_add_u32 m0, s62, 23552\n"
    "s_add_u32 s76, s76, 28672\n"
    "buffer_load_dwordx4 v2, s[28:31], s76 offen  lds\n"
    "global_load_dwordx2 v[224:225], v14, s[48:49] sc1\n"
    "s_waitcnt vmcnt(27)\n"
    "ds_read_b64_tr_b16 a[96:97], v4 offset:24576\n"
    "ds_read_b64_tr_b16 a[98:99], v4 offset:25088\n"
    "ds_read_b64_tr_b16 a[100:101], v5 offset:24576\n"
    "ds_read_b64_tr_b16 a[102:103], v5 offset:25088\n"
    "s_waitcnt vmcnt(25)\n"
    "ds_read_b64_tr_b16 a[104:105], v4 offset:26624\n"
    "ds_read_b64_tr_b16 a[106:107], v4 offset:27136\n"
    "ds_read_b64_tr_b16 a[108:109], v5 offset:26624\n"
    "ds_read_b64_tr_b16 a[110:111], v5 offset:27136\n"
    "s_waitcnt lgkmcnt(0)\n"
    "s_add_u32 m0, s62, 24576\n"
    "s_add_u32 s76, s76, 28672\n"
    "buffer_load_dwordx4 v2, s[28:31], s76 offen  lds\n"
    "s_add_u32 m0, s62, 25600\n"
    "s_add_u32 s76, s76, 28672\n"
    "buffer_load_dwordx4 v2, s[28:31], s76 offen  lds\n"
    "s_add_u32 m0, s62, 26624\n"
    "s_add_u32 s76, s76, 28672\n"
    "buffer_load_dwordx4 v2, s[28:31], s76 offen  lds\n"
    "s_add_u32 m0, s62, 27648\n"
    "s_add_u32 s76, s76, 28672\n"
    "buffer_load_dwordx4 v2, s[28:31], s76 offen  lds\n"
    "s_waitcnt vmcnt(27)\n"
    "ds_read_b64_tr_b16 a[112:113], v4 offset:0\n"
    "ds_read_b64_tr_b16 a[114:115], v4 offset:512\n"
    "ds_read_b64_tr_b16 a[116:117], v5 offset:0\n"
    "ds_read_b64_tr_b16 a[118:119], v5 offset:512\n"
    "s_waitcnt vmcnt(25)\n"
    "ds_read_b64_tr_b16 a[120:121], v4 offset:2048\n"
    "ds_read_b64_tr_b16 a[122:123], v4 offset:2560\n"
    "ds_read_b64_tr_b16 a[124:125], v5 offset:2048\n"
    "ds_read_b64_tr_b16 a[126:127], v5 offset:2560\n"
    "s_waitcnt lgkmcnt(0)\n"
    "s_add_u32 m0, s62, 0\n"
    "s_add_u32 s75, s75, 1\n"
    "s_cmp_eq_u32 s75, 8\n"
    "s_cselect_b32 s75, 0, s75\n"
    "s_mul_i32 s76, s75, 229376\n"
    "s_add_u32 s76, s76, s74\n"
    "buffer_load_dwordx4 v2, s[28:31], s76 offen  lds\n"
    "s_add_u32 m0, s62, 1024\n"
    "s_add_u32 s76, s76, 28672\n"
    "buffer_load_dwordx4 v2, s[28:31], s76 offen  lds\n"
    "s_add_u32 m0, s62, 2048\n"
    "s_add_u32 s76, s76, 28672\n"
    "buffer_load_dwordx4 v2, s[28:31], s76 offen  lds\n"
    "s_add_u32 m0, s62, 3072\n"
    "s_add_u32 s76, s76, 28672\n"
    "buffer_load_dwordx4 v2, s[28:31], s76 offen  lds\n"
    "s_waitcnt vmcnt(27)\n"
    "ds_read_b64_tr_b16 a[128:129], v4 offset:4096\n"
    "ds_read_b64_tr_b16 a[130:131], v4 offset:4608\n"
    "ds_read_b64_tr_b16 a[132:133], v5 offset:4096\n"
    "ds_read_b64_tr_b16 a[134:135], v5 offset:4608\n"
    "s_waitcnt vmcnt(25)\n"
    "ds_read_b64_tr_b16 a[136:137], v4 offset:6144\n"
    "ds_read_b64_tr_b16 a[138:139], v4 offset:6656\n"
    "ds_read_b64_tr_b16 a[140:141], v5 offset:6144\n"
    "ds_read_b64_tr_b16 a[142:143], v5 offset:6656\n"
    "s_waitcnt lgkmcnt(0)\n"
    "s_add_u32 m0, s62, 4096\n"
    "s_add_u32 s76, s76, 28672\n"
    "buffer_load_dwordx4 v2, s[28:31], s76 offen  lds\n"
    "s_add_u32 m0, s62, 5120\n"
    "s_add_u32 s76, s76, 28672\n"
    "buffer_load_dwordx4 v2, s[28:31], s76 offen  lds\n"
    "s_add_u32 m0, s62, 6144\n"
    "s_add_u32 s76, s76, 28672\n"
    "buffer_load_dwordx4 v2, s[28:31], s76 offen  lds\n"
    "s_add_u32 m0, s62, 7168\n"
    "s_add_u32 s76, s76, 28672\n"
    "buffer_load_dwordx4 v2, s[28:31], s76 offen  lds\n"
    "s_lshl_b32 s72, s55, 18\n"
    "s_lshl_b32 s64, s54, 16\n"
    "s_add_u32 s72, s72, s64\n"
    "s_mov_b32 s73, s81\n"
    "s_waitcnt vmcnt(12)\n"
    "s_mov_b32 s71, 0\n"
    "L_p1_%=:\n"
    "v_cmp_ne_u64 s[64:65], s[50:51], v[224:225]\n"
    "s_nop 1\n"
    "s_cmp_eq_u64 s[64:65], 0\n"
    "s_cbranch_scc1 L_p1_done_%=\n"
    "s_sleep 1\n"
    "global_load_dwordx2 v[224:225], v14, s[48:49] sc1\n"
    "s_add_u32 s71, s71, 1\n"
    "s_cmp_lt_u32 s71, 400000\n"
    "s_waitcnt vmcnt(0)\n"
    "s_cbranch_scc1 L_p1_%=\n"
    "L_p1_done_%=:\n"
    "s_lshl_b32 s63, s73, 13\n"
    "s_add_u32 s63, s63, s72\n"
    "buffer_load_dwordx4 v[96:99], v6, s[36:39], s63 offen offset:0 sc1\n"
    "ds_read_b64_tr_b16 a[144:145], v4 offset:8192\n"
    "ds_read_b64_tr_b16 a[146:147], v4 offset:8704\n"
    "ds_read_b64_tr_b16 a[148:149], v5 offset:8192\n"
    "ds_read_b64_tr_b16 a[150:151], v5 offset:8704\n"
    "buffer_load_dwordx4 v[100:103], v7, s[36:39], s63 offen offset:0 sc1\n"
    "ds_read_b64_tr_b16 a[152:153], v4 offset:10240\n"
    "ds_read_b64_tr_b16 a[154:155], v4 offset:10752\n"
    "ds_read_b64_tr_b16 a[156:157], v5 offset:10240\n"
    "ds_read_b64_tr_b16 a[158:159], v5 offset:10752\n"
    "buffer_load_dwordx4 v[104:107], v6, s[36:39], s63 offen offset:2048 sc1\n"
    "ds_read_b64_tr_b16 a[160:161], v4 offset:12288\n"
    "ds_read_b64_tr_b16 a[162:163], v4 offset:12800\n"
    "ds_read_b64_tr_b16 a[164:165], v5 offset:12288\n"
    "ds_read_b64_tr_b16 a[166:167], v5 offset:12800\n"
    "buffer_load_dwordx4 v[108:111], v7, s[36:39], s63 offen offset:2048 sc1\n"
    "ds_read_b64_tr_b16 a[168:169], v4 offset:14336\n"
    "ds_read_b64_tr_b16 a[170:171], v4 offset:14848\n"
    "ds_read_b64_tr_b16 a[172:173], v5 offset:14336\n"
    "ds_read_b64_tr_b16 a[174:175], v5 offset:14848\n"
    "s_add_u32 s63, s63, 4096\n"
    "buffer_load_dwordx4 v[112:115], v6, s[36:39], s63 offen offset:0 sc1\n"
    "ds_read_b64_tr_b16 a[176:177], v4 offset:16384\n"
    "ds_read_b64_tr_b16 a[178:179], v4 offset:16896\n"
    "ds_read_b64_tr_b16 a[180:181], v5 offset:16384\n"
    "ds_read_b64_tr_b16 a[182:183], v5 offset:16896\n"
    "buffer_load_dwordx4 v[116:119], v7, s[36:39], s63 offen offset:0 sc1\n"
    "ds_read_b64_tr_b16 a[184:185], v4 offset:18432\n"
    "ds_read_b64_tr_b16 a[186:187], v4 offset:18944\n"
    "ds_read_b64_tr_b16 a[188:189], v5 offset:18432\n"
    "ds_read_b64_tr_b16 a[190:191], v5 offset:18944\n"
    "buffer_load_dwordx4 v[120:123], v6, s[36:39], s63 offen offset:2048 sc1\n"
    "ds_read_b64_tr_b16 a[192:193], v4 offset:20480\n"
    "ds_read_b64_tr_b16 a[194:195], v4 offset:20992\n"
    "ds_read_b64_tr_b16 a[196:197], v5 offset:20480\n"
    "ds_read_b64_tr_b16 a[198:199], v5 offset:20992\n"
    "buffer_load_dwordx4 v[124:127], v7, s[36:39], s63 offen offset:2048 sc1\n"
    "ds_read_b64_tr_b16 a[200:201], v4 offset:22528\n"
    "ds_read_b64_tr_b16 a[202:203], v4 offset:23040\n"
    "ds_read_b64_tr_b16 a[204:205], v5 offset:22528\n"
    "ds_read_b64_tr_b16 a[206:207], v5 offset:23040\n"
    "s_add_u32 s73, s73, 1\n"
    "s_cmp_eq_u32 s73, 8\n"
    "s_cselect_b32 s73, 0, s73\n"
    "s_lshl_b32 s63, s73, 13\n"
    "s_add_u32 s63, s63, s72\n"
    "buffer_load_dwordx4 v[128:131], v6, s[36:39], s63 offen offset:0 sc1\n"
    "s_waitcnt vmcnt(19)\n"
    "ds_read_b64_tr_b16 a[208:209], v4 offset:24576\n"
    "ds_read_b64_tr_b16 a[210:211], v4 offset:25088\n"
    "ds_read_b64_tr_b16 a[212:213], v5 offset:24576\n"
    "ds_read_b64_tr_b16 a[214:215], v5 offset:25088\n"
    "buffer_load_dwordx4 v[132:135], v7, s[36:39], s63 offen offset:0 sc1\n"
    "s_waitcnt vmcnt(18)\n"
    "ds_read_b64_tr_b16 a[216:217], v4 offset:26624\n"
    "ds_read_b64_tr_b16 a[218:219], v4 offset:27136\n"
    "ds_read_b64_tr_b16 a[220:221], v5 offset:26624\n"
    "ds_read_b64_tr_b16 a[222:223], v5 offset:27136\n"
    "s_waitcnt vmcnt(16)\n"
    "ds_read_b64_tr_b16 a[224:225], v4 offset:0\n"
    "ds_read_b64_tr_b16 a[226:227], v4 offset:512\n"
    "ds_read_b64_tr_b16 a[228:229], v5 offset:0\n"
    "ds_read_b64_tr_b16 a[230:231], v5 offset:512\n"
    "s_waitcnt vmcnt(14)\n"
    "ds_read_b64_tr_b16 a[232:233], v4 offset:2048\n"
    "ds_read_b64_tr_b16 a[234:235], v4 offset:2560\n"
    "ds_read_b64_tr_b16 a[236:237], v5 offset:2048\n"
    "ds_read_b64_tr_b16 a[238:239], v5 offset:2560\n"
    "s_waitcnt vmcnt(12)\n"
    "ds_read_b64_tr_b16 a[240:241], v4 offset:4096\n"
    "ds_read_b64_tr_b16 a[242:243], v4 offset:4608\n"
    "ds_read_b64_tr_b16 a[244:245], v5 offset:4096\n"
    "ds_read_b64_tr_b16 a[246:247], v5 offset:4608\n"
    "s_waitcnt vmcnt(10)\n"
    "ds_read_b64_tr_b16 a[248:249], v4 offset:6144\n"
    "ds_read_b64_tr_b16 a[250:251], v4 offset:6656\n"
    "ds_read_b64_tr_b16 a[252:253], v5 offset:6144\n"
    "ds_read_b64_tr_b16 a[254:255], v5 offset:6656\n"
    "s_waitcnt lgkmcnt(0)\n"
    "s_waitcnt vmcnt(9)\n"
    "v_mfma_f32_32x32x16_bf16 v[32:47], a[0:3], v[96:99], v[32:47]\n"
    "v_mfma_f32_32x32x16_bf16 v[64:79], a[4:7], v[96:99], 0\n"
    "buffer_load_dwordx4 v[136:139], v6, s[36:39], s63 offen offset:2048 sc1\n"
    "s_waitcnt vmcnt(9)\n"
    "v_mfma_f32_32x32x16_bf16 v[48:63], a[0:3], v[100:103], 0\n"
    "v_mfma_f32_32x32x16_bf16 v[80:95], a[4:7], v[100:103], 0\n"
    "buffer_load_dwordx4 v[140:143], v7, s[36:39], s63 offen offset:2048 sc1\n"
    "s_waitcnt vmcnt(9)\n"
    "v_mfma_f32_32x32x16_bf16 v[32:47], a[8:11], v[104:107], v[32:47]\n"
    "v_mfma_f32_32x32x16_bf16 v[64:79], a[12:15], v[104:107], v[64:79]\n"
    "s_add_u32 s63, s63, 4096\n"
    "buffer_load_dwordx4 v[144:147], v6, s[36:39], s63 offen offset:0 sc1\n"
    "s_waitcnt vmcnt(9)\n"
    "v_mfma_f32_32x32x16_bf16 v[48:63], a[8:11], v[108:111], v[48:63]\n"
    "v_mfma_f32_32x32x16_bf16 v[80:95], a[12:15], v[108:111], v[80:95]\n"
    "buffer_load_dwordx4 v[148:151], v7, s[36:39], s63 offen offset:0 sc1\n"
    "s_waitcnt vmcnt(9)\n"
    "v_mfma_f32_32x32x16_bf16 v[32:47], a[16:19], v[112:115], v[32:47]\n"
    "v_mfma_f32_32x32x16_bf16 v[64:79], a[20:23], v[112:115], v[64:79]\n"
    "buffer_load_dwordx4 v[152:155], v6, s[36:39], s63 offen offset:2048 sc1\n"
    "s_waitcnt vmcnt(9)\n"
    "v_mfma_f32_32x32x16_bf16 v[48:63], a[16:19], v[116:119], v[48:63]\n"
    "v_mfma_f32_32x32x16_bf16 v[80:95], a[20:23], v[116:119], v[80:95]\n"
    "buffer_load_dwordx4 v[156:159], v7, s[36:39], s63 offen offset:2048 sc1\n"
    "s_waitcnt vmcnt(9)\n"
    "v_mfma_f32_32x32x16_bf16 v[32:47], a[24:27], v[120:123], v[32:47]\n"
    "v_mfma_f32_32x32x16_bf16 v[64:79], a[28:31], v[120:123], v[64:79]\n"
    "s_add_u32 s73, s73, 1\n"
    "s_cmp_eq_u32 s73, 8\n"
    "s_cselect_b32 s73, 0, s73\n"
    "s_lshl_b32 s63, s73, 13\n"
    "s_add_u32 s63, s63, s72\n"
    "buffer_load_dwordx4 v[160:163], v6, s[36:39], s63 offen offset:0 sc1\n"
    "s_waitcnt vmcnt(9)\n"
    "v_mfma_f32_32x32x16_bf16 v[48:63], a[24:27], v[124:127], v[48:63]\n"
    "v_mfma_f32_32x32x16_bf16 v[80:95], a[28:31], v[124:127], v[80:95]\n"
    "buffer_load_dwordx4 v[164:167], v7, s[36:39], s63 offen offset:0 sc1\n"
    "s_waitcnt vmcnt(9)\n"
    "v_mfma_f32_32x32x16_bf16 v[32:47], a[32:35], v[128:131], v[32:47]\n"
    "v_mfma_f32_32x32x16_bf16 v[64:79], a[36:39], v[128:131], v[64:79]\n"
    "buffer_load_dwordx4 v[168:171], v6, s[36:39], s63 offen offset:2048 sc1\n"
    "s_waitcnt vmcnt(9)\n"
    "v_mfma_f32_32x32x16_bf16 v[48:63], a[32:35], v[132:135], v[48:63]\n"
    "v_mfma_f32_32x32x16_bf16 v[80:95], a[36:39], v[132:135], v[80:95]\n"
    "buffer_load_dwordx4 v[172:175], v7, s[36:39], s63 offen offset:2048 sc1\n"
    "s_waitcnt vmcnt(9)\n"
    "v_mfma_f32_32x32x16_bf16 v[32:47], a[40:43], v[136:139], v[32:47]\n"
    "v_mfma_f32_32x32x16_bf16 v[64:79], a[44:47], v[136:139], v[64:79]\n"
    "s_add_u32 s63, s63, 4096\n"
    "buffer_load_dwordx4 v[176:179], v6, s[36:39], s63 offen offset:0 sc1\n"
    "s_waitcnt vmcnt(9)\n"
    "v_mfma_f32_32x32x16_bf16 v[48:63], a[40:43], v[140:143], v[48:63]\n"
    "v_mfma_f32_32x32x16_bf16 v[80:95], a[44:47], v[140:143], v[80:95]\n"
    "buffer_load_dwordx4 v[180:183], v7, s[36:39], s63 offen offset:0 sc1\n"
    "s_waitcnt vmcnt(9)\n"
    "v_mfma_f32_32x32x16_bf16 v[32:47], a[48:51], v[144:147], v[32:47]\n"
    "v_mfma_f32_32x32x16_bf16 v[64:79], a[52:55], v[144:147], v[64:79]\n"
    "buffer_load_dwordx4 v[184:187], v6, s[36:39], s63 offen offset:2048 sc1\n"
    "s_waitcnt vmcnt(9)\n"
    "v_mfma_f32_32x32x16_bf16 v[48:63], a[48:51], v[148:151], v[48:63]\n"
    "v_mfma_f32_32x32x16_bf16 v[80:95], a[52:55], v[148:151], v[80:95]\n"
    "buffer_load_dwordx4 v[188:191], v7, s[36:39], s63 offen offset:2048 sc1\n"
    "s_waitcnt vmcnt(9)\n"
    "v_mfma_f32_32x32x16_bf16 v[32:47], a[56:59], v[152:155], v[32:47]\n"
    "v_mfma_f32_32x32x16_bf16 v[64:79], a[60:63], v[152:155], v[64:79]\n"
    "s_add_u32 s73, s73, 1\n"
    "s_cmp_eq_u32 s73, 8\n"
    "s_cselect_b32 s73, 0, s73\n"
    "s_lshl_b32 s63, s73, 13\n"
    "s_add_u32 s63, s63, s72\n"
    "buffer_load_dwordx4 v[192:195], v6, s[36:39], s63 offen offset:0 sc1\n"
    "s_waitcnt vmcnt(9)\n"
    "v_mfma_f32_32x32x16_bf16 v[48:63], a[56:59], v[156:159], v[48:63]\n"
    "v_mfma_f32_32x32x16_bf16 v[80:95], a[60:63], v[156:159], v[80:95]\n"
    "buffer_load_dwordx4 v[196:199], v7, s[36:39], s63 offen offset:0 sc1\n"
    "s_waitcnt vmcnt(9)\n"
    "v_mfma_f32_32x32x16_bf16 v[32:47], a[64:67], v[160:163], v[32:47]\n"
    "v_mfma_f32_32x32x16_bf16 v[64:79], a[68:71], v[160:163], v[64:79]\n"
    "buffer_load_dwordx4 v[200:203], v6, s[36:39], s63 offen offset:2048 sc1\n"
    "s_waitcnt vmcnt(9)\n"
    "v_mfma_f32_32x32x16_bf16 v[48:63], a[64:67], v[164:167], v[48:63]\n"
    "v_mfma_f32_32x32x16_bf16 v[80:95], a[68:71], v[164:167], v[80:95]\n"
    "buffer_load_dwordx4 v[204:207], v7, s[36:39], s63 offen offset:2048 sc1\n"
    "s_waitcnt vmcnt(9)\n"
    "v_mfma_f32_32x32x16_bf16 v[32:47], a[72:75], v[168:171], v[32:47]\n"
    "v_mfma_f32_32x32x16_bf16 v[64:79], a[76:79], v[168:171], v[64:79]\n"
    "s_add_u32 s63, s63, 4096\n"
    "buffer_load_dwordx4 v[96:99], v6, s[36:39], s63 offen offset:0 sc1\n"
    "s_waitcnt vmcnt(9)\n"
    "v_mfma_f32_32x32x16_bf16 v[48:63], a[72:75], v[172:175], v[48:63]\n"
    "v_mfma_f32_32x32x16_bf16 v[80:95], a[76:79], v[172:175], v[80:95]\n"
    "buffer_load_dwordx4 v[100:103], v7, s[36:39], s63 offen offset:0 sc1\n"
    "s_waitcnt vmcnt(9)\n"
    "v_mfma_f32_32x32x16_bf16 v[32:47], a[80:83], v[176:179], v[32:47]\n"
    "v_mfma_f32_32x32x16_bf16 v[64:79], a[84:87], v[176:179], v[64:79]\n"
    "buffer_load_dwordx4 v[104:107], v6, s[36:39], s63 offen offset:2048 sc1\n"
    "s_waitcnt vmcnt(9)\n"
    "v_mfma_f32_32x32x16_bf16 v[48:63], a[80:83], v[180:183], v[48:63]\n"
    "v_mfma_f32_32x32x16_bf16 v[80:95], a[84:87], v[180:183], v[80:95]\n"
    "buffer_load_dwordx4 v[108:111], v7, s[36:39], s63 offen offset:2048 sc1\n"
    "s_waitcnt vmcnt(9)\n"
    "v_mfma_f32_32x32x16_bf16 v[32:47], a[88:91], v[184:187], v[32:47]\n"
    "v_mfma_f32_32x32x16_bf16 v[64:79], a[92:95], v[184:187], v[64:79]\n"
    "s_add_u32 s73, s73, 1\n"
    "s_cmp_eq_u32 s73, 8\n"
    "s_cselect_b32 s73, 0, s73\n"
    "s_lshl_b32 s63, s73, 13\n"
    "s_add_u32 s63, s63, s72\n"
    "buffer_load_dwordx4 v[112:115], v6, s[36:39], s63 offen offset:0 sc1\n"
    "s_waitcnt vmcnt(9)\n"
    "v_mfma_f32_32x32x16_bf16 v[48:63], a[88:91], v[188:191], v[48:63]\n"
    "v_mfma_f32_32x32x16_bf16 v[80:95], a[92:95], v[188:191], v[80:95]\n"
    "buffer_load_dwordx4 v[116:119], v7, s[36:39], s63 offen offset:0 sc1\n"
    "s_waitcnt vmcnt(9)\n"
    "v_mfma_f32_32x32x16_bf16 v[32:47], a[96:99], v[192:195], v[32:47]\n"
    "v_mfma_f32_32x32x16_bf16 v[64:79], a[100:103], v[192:195], v[64:79]\n"
    "buffer_load_dwordx4 v[120:123], v6, s[36:39], s63 offen offset:2048 sc1\n"
    "s_waitcnt vmcnt(9)\n"
    "v_mfma_f32_32x32x16_bf16 v[48:63], a[96:99], v[196:199], v[48:63]\n"
    "v_mfma_f32_32x32x16_bf16 v[80:95], a[100:103], v[196:199], v[80:95]\n"
    "buffer_load_dwordx4 v[124:127], v7, s[36:39], s63 offen offset:2048 sc1\n"
    "s_waitcnt vmcnt(9)\n"
    "v_mfma_f32_32x32x16_bf16 v[32:47], a[104:107], v[200:203], v[32:47]\n"
    "v_mfma_f32_32x32x16_bf16 v[64:79], a[108:111], v[200:203], v[64:79]\n"
    "s_add_u32 s63, s63, 4096\n"
    "buffer_load_dwordx4 v[128:131], v6, s[36:39], s63 offen offset:0 sc1\n"
    "s_waitcnt vmcnt(9)\n"
    "v_mfma_f32_32x32x16_bf16 v[48:63], a[104:107], v[204:207], v[48:63]\n"
    "v_mfma_f32_32x32x16_bf16 v[80:95], a[108:111], v[204:207], v[80:95]\n"
    "buffer_load_dwordx4 v[132:135], v7, s[36:39], s63 offen offset:0 sc1\n"
    "s_waitcnt vmcnt(9)\n"
    "v_mfma_f32_32x32x16_bf16 v[32:47], a[112:115], v[96:99], v[32:47]\n"
    "v_mfma_f32_32x32x16_bf16 v[64:79], a[116:119], v[96:99], v[64:79]\n"
    "buffer_load_dwordx4 v[136:139], v6, s[36:39], s63 offen offset:2048 sc1\n"
    "s_waitcnt vmcnt(9)\n"
    "v_mfma_f32_32x32x16_bf16 v[48:63], a[112:115], v[100:103], v[48:63]\n"
    "v_mfma_f32_32x32x16_bf16 v[80:95], a[116:119], v[100:103], v[80:95]\n"
    "buffer_load_dwordx4 v[140:143], v7, s[36:39], s63 offen offset:2048 sc1\n"
    "s_waitcnt vmcnt(9)\n"
    "v_mfma_f32_32x32x16_bf16 v[32:47], a[120:123], v[104:107], v[32:47]\n"
    "v_mfma_f32_32x32x16_bf16 v[64:79], a[124:127], v[104:107], v[64:79]\n"
    "s_add_u32 s73, s73, 1\n"
    "s_cmp_eq_u32 s73, 8\n"
    "s_cselect_b32 s73, 0, s73\n"
    "s_lshl_b32 s63, s73, 13\n"
    "s_add_u32 s63, s63, s72\n"
    "buffer_load_dwordx4 v[144:147], v6, s[36:39], s63 offen offset:0 sc1\n"
    "s_waitcnt vmcnt(9)\n"
    "v_mfma_f32_32x32x16_bf16 v[48:63], a[120:123], v[108:111], v[48:63]\n"
    "v_mfma_f32_32x32x16_bf16 v[80:95], a[124:127], v[108:111], v[80:95]\n"
    "buffer_load_dwordx4 v[148:151], v7, s[36:39], s63 offen offset:0 sc1\n"
    "s_waitcnt vmcnt(9)\n"
    "v_mfma_f32_32x32x16_bf16 v[32:47], a[128:131], v[112:115], v[32:47]\n"
    "v_mfma_f32_32x32x16_bf16 v[64:79], a[132:135], v[112:115], v[64:79]\n"
    "buffer_load_dwordx4 v[152:155], v6, s[36:39], s63 offen offset:2048 sc1\n"
    "s_waitcnt vmcnt(9)\n"
    "v_mfma_f32_32x32x16_bf16 v[48:63], a[128:131], v[116:119], v[48:63]\n"
    "v_mfma_f32_32x32x16_bf16 v[80:95], a[132:135], v[116:119], v[80:95]\n"
    "buffer_load_dwordx4 v[156:159], v7, s[36:39], s63 offen offset:2048 sc1\n"
    "s_waitcnt vmcnt(9)\n"
    "v_mfma_f32_32x32x16_bf16 v[32:47], a[136:139], v[120:123], v[32:47]\n"
    "v_mfma_f32_32x32x16_bf16 v[64:79], a[140:143], v[120:123], v[64:79]\n"
    "s_add_u32 s63, s63, 4096\n"
    "buffer_load_dwordx4 v[160:163], v6, s[36:39], s63 offen offset:0 sc1\n"
    "s_waitcnt vmcnt(9)\n"
    "v_mfma_f32_32x32x16_bf16 v[48:63], a[136:139], v[124:127], v[48:63]\n"
    "v_mfma_f32_32x32x16_bf16 v[80:95], a[140:143], v[124:127], v[80:95]\n"
    "buffer_load_dwordx4 v[164:167], v7, s[36:39], s63 offen offset:0 sc1\n"
    "s_waitcnt vmcnt(9)\n"
    "v_mfma_f32_32x32x16_bf16 v[32:47], a[144:147], v[128:131], v[32:47]\n"
    "v_mfma_f32_32x32x16_bf16 v[64:79], a[148:151], v[128:131], v[64:79]\n"
    "buffer_load_dwordx4 v[168:171], v6, s[36:39], s63 offen offset:2048 sc1\n"
    "s_waitcnt vmcnt(9)\n"
    "v_mfma_f32_32x32x16_bf16 v[48:63], a[144:147], v[132:135], v[48:63]\n"
    "v_mfma_f32_32x32x16_bf16 v[80:95], a[148:151], v[132:135], v[80:95]\n"
    "buffer_load_dwordx4 v[172:175], v7, s[36:39], s63 offen offset:2048 sc1\n"
    "s_waitcnt vmcnt(9)\n"
    "v_mfma_f32_32x32x16_bf16 v[32:47], a[152:155], v[136:139], v[32:47]\n"
    "v_mfma_f32_32x32x16_bf16 v[64:79], a[156:159], v[136:139], v[64:79]\n"
    "s_add_u32 s73, s73, 1\n"
    "s_cmp_eq_u32 s73, 8\n"
    "s_cselect_b32 s73, 0, s73\n"
    "s_lshl_b32 s63, s73, 13\n"
    "s_add_u32 s63, s63, s72\n"
    "buffer_load_dwordx4 v[176:179], v6, s[36:39], s63 offen offset:0 sc1\n"
    "s_waitcnt vmcnt(9)\n"
    "v_mfma_f32_32x32x16_bf16 v[48:63], a[152:155], v[140:143], v[48:63]\n"
    "v_mfma_f32_32x32x16_bf16 v[80:95], a[156:159], v[140:143], v[80:95]\n"
    "buffer_load_dwordx4 v[180:183], v7, s[36:39], s63 offen offset:0 sc1\n"
    "s_waitcnt vmcnt(9)\n"
    "v_mfma_f32_32x32x16_bf16 v[32:47], a[160:163], v[144:147], v[32:47]\n"
    "v_mfma_f32_32x32x16_bf16 v[64:79], a[164:167], v[144:147], v[64:79]\n"
    "buffer_load_dwordx4 v[184:187], v6, s[36:39], s63 offen offset:2048 sc1\n"
    "s_waitcnt vmcnt(9)\n"
    "v_mfma_f32_32x32x16_bf16 v[48:63], a[160:163], v[148:151], v[48:63]\n"
    "v_mfma_f32_32x32x16_bf16 v[80:95], a[164:167], v[148:151], v[80:95]\n"
    "buffer_load_dwordx4 v[188:191], v7, s[36:39], s63 offen offset:2048 sc1\n"
    "s_waitcnt vmcnt(9)\n"
    "v_mfma_f32_32x32x16_bf16 v[32:47], a[168:171], v[152:155], v[32:47]\n"
    "v_mfma_f32_32x32x16_bf16 v[64:79], a[172:175], v[152:155], v[64:79]\n"
    "s_add_u32 s63, s63, 4096\n"
    "buffer_load_dwordx4 v[192:195], v6, s[36:39], s63 offen offset:0 sc1\n"
    "s_waitcnt vmcnt(9)\n"
    "v_mfma_f32_32x32x16_bf16 v[48:63], a[168:171], v[156:159], v[48:63]\n"
    "v_mfma_f32_32x32x16_bf16 v[80:95], a[172:175], v[156:159], v[80:95]\n"
    "buffer_load_dwordx4 v[196:199], v7, s[36:39], s63 offen offset:0 sc1\n"
    "s_waitcnt vmcnt(9)\n"
    "v_mfma_f32_32x32x16_bf16 v[32:47], a[176:179], v[160:163], v[32:47]\n"
    "v_mfma_f32_32x32x16_bf16 v[64:79], a[180:183], v[160:163], v[64:79]\n"
    "buffer_load_dwordx4 v[200:203], v6, s[36:39], s63 offen offset:2048 sc1\n"
    "s_waitcnt vmcnt(9)\n"
    "v_mfma_f32_32x32x16_bf16 v[48:63], a[176:179], v[164:167], v[48:63]\n"
    "v_mfma_f32_32x32x16_bf16 v[80:95], a[180:183], v[164:167], v[80:95]\n"
    "buffer_load_dwordx4 v[204:207], v7, s[36:39], s63 offen offset:2048 sc1\n"
    "s_waitcnt vmcnt(9)\n"
    "v_mfma_f32_32x32x16_bf16 v[32:47], a[184:187], v[168:171], v[32:47]\n"
    "v_mfma_f32_32x32x16_bf16 v[64:79], a[188:191], v[168:171], v[64:79]\n"
    "s_add_u32 s73, s73, 1\n"
    "s_cmp_eq_u32 s73, 8\n"
    "s_cselect_b32 s73, 0, s73\n"
    "s_lshl_b32 s63, s73, 13\n"
    "s_add_u32 s63, s63, s72\n"
    "s_add_u32 s83, s63, 4096\n"
    "buffer_load_dwordx4 v[100:103], v7, s[36:39], s63 offen offset:0 sc1\n"
    "s_waitcnt vmcnt(9)\n"
    "v_mfma_f32_32x32x16_bf16 v[48:63], a[184:187], v[172:175], v[48:63]\n"
    "v_mfma_f32_32x32x16_bf16 v[80:95], a[188:191], v[172:175], v[80:95]\n"
    "buffer_load_dwordx4 v[108:111], v7, s[36:39], s63 offen offset:2048 sc1\n"
    "s_waitcnt vmcnt(9)\n"
    "v_mfma_f32_32x32x16_bf16 v[32:47], a[192:195], v[176:179], v[32:47]\n"
    "v_mfma_f32_32x32x16_bf16 v[64:79], a[196:199], v[176:179], v[64:79]\n"
    "buffer_load_dwordx4 v[116:119], v7, s[36:39], s83 offen offset:0 sc1\n"
    "s_waitcnt vmcnt(9)\n"
    "v_mfma_f32_32x32x16_bf16 v[48:63], a[192:195], v[180:183], v[48:63]\n"
    "v_mfma_f32_32x32x16_bf16 v[80:95], a[196:199], v[180:183], v[80:95]\n"
    "buffer_load_dwordx4 v[124:127], v7, s[36:39], s83 offen offset:2048 sc1\n"
    "s_waitcnt vmcnt(9)\n"
    "v_mfma_f32_32x32x16_bf16 v[32:47], a[200:203], v[184:187], v[32:47]\n"
    "v_mfma_f32_32x32x16_bf16 v[64:79], a[204:207], v[184:187], v[64:79]\n"
    "buffer_load_dwordx4 v[96:99], v6, s[36:39], s63 offen offset:0 sc1\n"
    "s_waitcnt vmcnt(9)\n"
    "v_mfma_f32_32x32x16_bf16 v[48:63], a[200:203], v[188:191], v[48:63]\n"
    "v_mfma_f32_32x32x16_bf16 v[80:95], a[204:207], v[188:191], v[80:95]\n"
    "buffer_load_dwordx4 v[104:107], v6, s[36:39], s63 offen offset:2048 sc1\n"
    "s_waitcnt vmcnt(9)\n"
    "v_mfma_f32_32x32x16_bf16 v[32:47], a[208:211], v[192:195], v[32:47]\n"
    "v_mfma_f32_32x32x16_bf16 v[64:79], a[212:215], v[192:195], v[64:79]\n"
    "buffer_load_dwordx4 v[112:115], v6, s[36:39], s83 offen offset:0 sc1\n"
    "s_waitcnt vmcnt(9)\n"
    "v_mfma_f32_32x32x16_bf16 v[48:63], a[208:211], v[196:199], v[48:63]\n"
    "v_mfma_f32_32x32x16_bf16 v[80:95], a[212:215], v[196:199], v[80:95]\n"
    "buffer_load_dwordx4 v[120:123], v6, s[36:39], s83 offen offset:2048 sc1\n"
    "s_waitcnt vmcnt(9)\n"
    "v_mfma_f32_32x32x16_bf16 v[32:47], a[216:219], v[200:203], v[32:47]\n"
    "v_mfma_f32_32x32x16_bf16 v[64:79], a[220:223], v[200:203], v[64:79]\n"
    "s_add_u32 m0, s62, 0\n"
    "s_mul_i32 s80, s79, 262144\n"
    "s_add_u32 s80, s80, s78\n"
    "buffer_load_dwordx4 v3, s[96:99], s80 offen  lds\n"
    "s_waitcnt vmcnt(9)\n"
    "v_mfma_f32_32x32x16_bf16 v[48:63], a[216:219], v[204:207], v[48:63]\n"
    "v_mfma_f32_32x32x16_bf16 v[80:95], a[220:223], v[204:207], v[80:95]\n"
    "s_add_u32 m0, s62, 1024\n"
    "s_add_u32 s80, s80, 16384\n"
    "buffer_load_dwordx4 v3, s[96:99], s80 offen  lds\n"
    "s_waitcnt vmcnt(9)\n"
    "v_mfma_f32_32x32x16_bf16 v[48:63], a[224:227], v[100:103], v[48:63]\n"
    "v_mfma_f32_32x32x16_bf16 v[80:95], a[228:231], v[100:103], v[80:95]\n"
    "s_add_u32 m0, s62, 2048\n"
    "s_add_u32 s80, s80, 49152\n"
    "buffer_load_dwordx4 v3, s[96:99], s80 offen  lds\n"
    "s_waitcnt vmcnt(9)\n"
    "v_mfma_f32_32x32x16_bf16 v[48:63], a[232:235], v[108:111], v[48:63]\n"
    "v_mfma_f32_32x32x16_bf16 v[80:95], a[236:239], v[108:111], v[80:95]\n"
    "s_add_u32 m0, s62, 3072\n"
    "s_add_u32 s80, s80, 16384\n"
    "buffer_load_dwordx4 v3, s[96:99], s80 offen  lds\n"
    "s_waitcnt vmcnt(9)\n"
    "v_mfma_f32_32x32x16_bf16 v[48:63], a[240:243], v[116:119], v[48:63]\n"
    "v_mfma_f32_32x32x16_bf16 v[80:95], a[244:247], v[116:119], v[80:95]\n"
    "s_add_u32 m0, s62, 4096\n"
    "s_add_u32 s80, s80, 49152\n"
    "buffer_load_dwordx4 v3, s[96:99], s80 offen  lds\n"
    "s_waitcnt vmcnt(9)\n"
    "v_mfma_f32_32x32x16_bf16 v[48:63], a[248:251], v[124:127], v[48:63]\n"
    "v_mfma_f32_32x32x16_bf16 v[80:95], a[252:255], v[124:127], v[80:95]\n"
    "s_add_u32 m0, s62, 5120\n"
    "s_add_u32 s80, s80, 16384\n"
    "buffer_load_dwordx4 v3, s[96:99], s80 offen  lds\n"
    "s_waitcnt vmcnt(9)\n"
    "v_mfma_f32_32x32x16_bf16 v[64:79], a[228:231], v[96:99], v[64:79]\n"
    "s_add_u32 m0, s62, 6144\n"
    "s_add_u32 s80, s80, 49152\n"
    "buffer_load_dwordx4 v3, s[96:99], s80 offen  lds\n"
    "s_waitcnt vmcnt(9)\n"
    "v_mfma_f32_32x32x16_bf16 v[64:79], a[236:239], v[104:107], v[64:79]\n"
    "s_add_u32 m0, s62, 7168\n"
    "s_add_u32 s80, s80, 16384\n"
    "buffer_load_dwordx4 v3, s[96:99], s80 offen  lds\n"
    "s_waitcnt vmcnt(9)\n"
    "v_mfma_f32_32x32x16_bf16 v[64:79], a[244:247], v[112:115], v[64:79]\n"
    "s_waitcnt vmcnt(8)\n"
    "v_mfma_f32_32x32x16_bf16 v[64:79], a[252:255], v[120:123], v[64:79]\n"
    "ds_write_b128 v8, v[48:51] offset:0\n"
    "ds_write_b128 v8, v[52:55] offset:1024\n"
    "ds_write_b128 v8, v[56:59] offset:2048\n"
    "ds_write_b128 v8, v[60:63] offset:3072\n"
    "v_mfma_f32_32x32x16_bf16 v[32:47], a[224:227], v[96:99], v[32:47]\n"
    "v_mfma_f32_32x32x16_bf16 v[32:47], a[232:235], v[104:107], v[32:47]\n"
    "v_mfma_f32_32x32x16_bf16 v[32:47], a[240:243], v[112:115], v[32:47]\n"
    "v_mfma_f32_32x32x16_bf16 v[32:47], a[248:251], v[120:123], v[32:47]\n"
    "ds_write_b128 v10, v[80:83] offset:0\n"
    "ds_write_b128 v10, v[84:87] offset:1024\n"
    "ds_write_b128 v10, v[88:91] offset:2048\n"
    "ds_write_b128 v10, v[92:95] offset:3072\n"
    "ds_write_b128 v9, v[64:67] offset:0\n"
    "ds_write_b128 v9, v[68:71] offset:1024\n"
    "ds_write_b128 v9, v[72:75] offset:2048\n"
    "ds_write_b128 v9, v[76:79] offset:3072\n"
    "s_waitcnt lgkmcnt(0)\n"
    "s_barrier\n"
    "ds_read_b128 v[96:99], v11 offset:0\n"
    "ds_read_b128 v[100:103], v11 offset:1024\n"
    "ds_read_b128 v[104:107], v11 offset:2048\n"
    "ds_read_b128 v[108:111], v11 offset:3072\n"
    "ds_read_b128 v[112:115], v11 offset:4096\n"
    "ds_read_b128 v[116:119], v11 offset:5120\n"
    "ds_read_b128 v[120:123], v11 offset:6144\n"
    "ds_read_b128 v[124:127], v11 offset:7168\n"
    "ds_read_b128 v[128:131], v11 offset:8192\n"
    "ds_read_b128 v[132:135], v11 offset:9216\n"
    "ds_read_b128 v[136:139], v11 offset:10240\n"
    "ds_read_b128 v[140:143], v11 offset:11264\n"
    "s_waitcnt lgkmcnt(8)\n"
    "v_pk_add_f32 v[32:33], v[32:33], v[96:97]\n"
    "v_pk_add_f32 v[34:35], v[34:35], v[98:99]\n"
    "v_pk_add_f32 v[36:37], v[36:37], v[100:101]\n"
    "v_pk_add_f32 v[38:39], v[38:39], v[102:103]\n"
    "v_pk_add_f32 v[40:41], v[40:41], v[104:105]\n"
    "v_pk_add_f32 v[42:43], v[42:43], v[106:107]\n"
    "v_pk_add_f32 v[44:45], v[44:45], v[108:109]\n"
    "v_pk_add_f32 v[46:47], v[46:47], v[110:111]\n"
    "s_waitcnt lgkmcnt(4)\n"
    "v_pk_add_f32 v[32:33], v[32:33], v[112:113]\n"
    "v_pk_add_f32 v[34:35], v[34:35], v[114:115]\n"
    "v_pk_add_f32 v[36:37], v[36:37], v[116:117]\n"
    "v_pk_add_f32 v[38:39], v[38:39], v[118:119]\n"
    "v_pk_add_f32 v[40:41], v[40:41], v[120:121]\n"
    "v_pk_add_f32 v[42:43], v[42:43], v[122:123]\n"
    "v_pk_add_f32 v[44:45], v[44:45], v[124:125]\n"
    "v_pk_add_f32 v[46:47], v[46:47], v[126:127]\n"
    "s_waitcnt lgkmcnt(0)\n"
    "v_pk_add_f32 v[32:33], v[32:33], v[128:129]\n"
    "v_pk_add_f32 v[34:35], v[34:35], v[130:131]\n"
    "v_pk_add_f32 v[36:37], v[36:37], v[132:133]\n"
    "v_pk_add_f32 v[38:39], v[38:39], v[134:135]\n"
    "v_pk_add_f32 v[40:41], v[40:41], v[136:137]\n"
    "v_pk_add_f32 v[42:43], v[42:43], v[138:139]\n"
    "v_pk_add_f32 v[44:45], v[44:45], v[140:141]\n"
    "v_pk_add_f32 v[46:47], v[46:47], v[142:143]\n"
    "s_mul_i32 s64, s55, 112\n"
    "s_lshl_b32 s65, s56, 2\n"
    "s_add_u32 s64, s64, s65\n"
    "s_and_b32 s65, s54, 2\n"
    "s_add_u32 s64, s64, s65\n"
    "s_lshl_b32 s64, s64, 1\n"
    "s_and_b32 s65, s54, 1\n"
    "s_add_u32 s64, s64, s65\n"
    "s_lshl_b32 s66, s64, 10\n"
    "v_pk_mul_f32 v[144:145], v[32:33], v[32:33]\n"
    "v_pk_mul_f32 v[146:147], v[34:35], v[34:35]\n"
    "v_pk_mul_f32 v[148:149], v[36:37], v[36:37]\n"
    "v_pk_mul_f32 v[150:151], v[38:39], v[38:39]\n"
    "v_pk_mul_f32 v[152:153], v[40:41], v[40:41]\n"
    "v_pk_mul_f32 v[154:155], v[42:43], v[42:43]\n"
    "v_pk_mul_f32 v[156:157], v[44:45], v[44:45]\n"
    "v_pk_mul_f32 v[158:159], v[46:47], v[46:47]\n"
    "v_pk_fma_f32 v[144:145], v[144:145], v[220:221], v[218:219]\n"
    "v_pk_fma_f32 v[146:147], v[146:147], v[220:221], v[218:219]\n"
    "v_pk_fma_f32 v[148:149], v[148:149], v[220:221], v[218:219]\n"
    "v_pk_fma_f32 v[150:151], v[150:151], v[220:221], v[218:219]\n"
    "s_add_u32 m0, s62, 8192\n"
    "s_add_u32 s79, s79, 1\n"
    "s_cmp_eq_u32 s79, 7\n"
    "s_cselect_b32 s79, 0, s79\n"
    "s_mul_i32 s80, s79, 262144\n"
    "s_add_u32 s80, s80, s78\n"
    "buffer_load_dwordx4 v3, s[96:99], s80 offen  lds\n"
    "v_pk_fma_f32 v[152:153], v[152:153], v[220:221], v[218:219]\n"
    "v_pk_fma_f32 v[154:155], v[154:155], v[220:221], v[218:219]\n"
    "v_pk_fma_f32 v[156:157], v[156:157], v[220:221], v[218:219]\n"
    "v_pk_fma_f32 v[158:159], v[158:159], v[220:221], v[218:219]\n"
    "v_pk_mul_f32 v[160:161], v[144:145], v[32:33]\n"
    "v_pk_mul_f32 v[162:163], v[146:147], v[34:35]\n"
    "v_pk_mul_f32 v[164:165], v[148:149], v[36:37]\n"
    "v_pk_mul_f32 v[166:167], v[150:151], v[38:39]\n"
    "v_pk_mul_f32 v[168:169], v[152:153], v[40:41]\n"
    "v_pk_mul_f32 v[170:171], v[154:155], v[42:43]\n"
    "v_pk_mul_f32 v[172:173], v[156:157], v[44:45]\n"
    "v_pk_mul_f32 v[174:175], v[158:159], v[46:47]\n"
    "s_add_u32 m0, s62, 9216\n"
    "s_add_u32 s80, s80, 16384\n"
    "buffer_load_dwordx4 v3, s[96:99], s80 offen  lds\n"
    "v_exp_f32 v160, v160\n"
    "v_exp_f32 v161, v161\n"
    "v_exp_f32 v162, v162\n"
    "v_exp_f32 v163, v163\n"
    "v_exp_f32 v164, v164\n"
    "v_exp_f32 v165, v165\n"
    "v_exp_f32 v166, v166\n"
    "v_exp_f32 v167, v167\n"
    "v_exp_f32 v168, v168\n"
    "v_exp_f32 v169, v169\n"
    "v_exp_f32 v170, v170\n"
    "v_exp_f32 v171, v171\n"
    "s_add_u32 m0, s62, 10240\n"
    "s_add_u32 s80, s80, 49152\n"
    "buffer_load_dwordx4 v3, s[96:99], s80 offen  lds\n"
    "v_exp_f32 v172, v172\n"
    "v_exp_f32 v173, v173\n"
    "v_exp_f32 v174, v174\n"
    "v_exp_f32 v175, v175\n"
    "v_pk_add_f32 v[160:161], v[160:161], v[222:223]\n"
    "v_pk_add_f32 v[162:163], v[162:163], v[222:223]\n"
    "v_pk_add_f32 v[164:165], v[164:165], v[222:223]\n"
    "v_pk_add_f32 v[166:167], v[166:167], v[222:223]\n"
    "v_pk_add_f32 v[168:169], v[168:169], v[222:223]\n"
    "v_pk_add_f32 v[170:171], v[170:171], v[222:223]\n"
    "v_pk_add_f32 v[172:173], v[172:173], v[222:223]\n"
    "v_pk_add_f32 v[174:175], v[174:175], v[222:223]\n"
    "s_add_u32 m0, s62, 11264\n"
    "s_add_u32 s80, s80, 16384\n"
    "buffer_load_dwordx4 v3, s[96:99], s80 offen  lds\n"
    "v_rcp_f32 v160, v160\n"
    "v_rcp_f32 v161, v161\n"
    "v_rcp_f32 v162, v162\n"
    "v_rcp_f32 v163, v163\n"
    "v_rcp_f32 v164, v164\n"
    "v_rcp_f32 v165, v165\n"
    "v_rcp_f32 v166, v166\n"
    "v_rcp_f32 v167, v167\n"
    "v_rcp_f32 v168, v168\n"
    "v_rcp_f32 v169, v169\n"
    "v_rcp_f32 v170, v170\n"
    "v_rcp_f32 v171, v171\n"
    "v_rcp_f32 v172, v172\n"
    "v_rcp_f32 v173, v173\n"
    "v_rcp_f32 v174, v174\n"
    "v_rcp_f32 v175, v175\n"
    "v_pk_mul_f32 v[160:161], v[160:161], v[32:33]\n"
    "v_pk_mul_f32 v[162:163], v[162:163], v[34:35]\n"
    "v_pk_mul_f32 v[164:165], v[164:165], v[36:37]\n"
    "v_pk_mul_f32 v[166:167], v[166:167], v[38:39]\n"
    "v_pk_mul_f32 v[168:169], v[168:169], v[40:41]\n"
    "v_pk_mul_f32 v[170:171], v[170:171], v[42:43]\n"
    "v_pk_mul_f32 v[172:173], v[172:173], v[44:45]\n"
    "v_pk_mul_f32 v[174:175], v[174:175], v[46:47]\n"
    "v_cvt_pk_bf16_f32 v176, v160, v161\n"
    "v_cvt_pk_bf16_f32 v177, v162, v163\n"
    "v_cvt_pk_bf16_f32 v178, v164, v165\n"
    "v_cvt_pk_bf16_f32 v179, v166, v167\n"
    "v_cvt_pk_bf16_f32 v180, v168, v169\n"
    "v_cvt_pk_bf16_f32 v181, v170, v171\n"
    "v_cvt_pk_bf16_f32 v182, v172, v173\n"
    "v_cvt_pk_bf16_f32 v183, v174, v175\n"
    "buffer_store_dwordx4 v[176:179], v12, s[40:43], s66 offen sc1\n"
    "buffer_store_dwordx4 v[180:183], v12, s[40:43], s66 offen offset:2048 sc1\n"
    "s_add_u32 m0, s62, 12288\n"
    "s_add_u32 s80, s80, 49152\n"
    "buffer_load_dwordx4 v3, s[96:99], s80 offen  lds\n"
    "s_add_u32 m0, s62, 13312\n"
    "s_add_u32 s80, s80, 16384\n"
    "buffer_load_dwordx4 v3, s[96:99], s80 offen  lds\n"
    "s_add_u32 m0, s62, 14336\n"
    "s_add_u32 s80, s80, 49152\n"
    "buffer_load_dwordx4 v3, s[96:99], s80 offen  lds\n"
    "s_add_u32 m0, s62, 15360\n"
    "s_add_u32 s80, s80, 16384\n"
    "buffer_load_dwordx4 v3, s[96:99], s80 offen  lds\n"
    "s_add_u32 m0, s62, 16384\n"
    "s_add_u32 s79, s79, 1\n"
    "s_cmp_eq_u32 s79, 7\n"
    "s_cselect_b32 s79, 0, s79\n"
    "s_mul_i32 s80, s79, 262144\n"
    "s_add_u32 s80, s80, s78\n"
    "buffer_load_dwordx4 v3, s[96:99], s80 offen  lds\n"
    "s_add_u32 m0, s62, 17408\n"
    "s_add_u32 s80, s80, 16384\n"
    "buffer_load_dwordx4 v3, s[96:99], s80 offen  lds\n"
    "s_add_u32 m0, s62, 18432\n"
    "s_add_u32 s80, s80, 49152\n"
    "buffer_load_dwordx4 v3, s[96:99], s80 offen  lds\n"
    "s_add_u32 m0, s62, 19456\n"
    "s_add_u32 s80, s80, 16384\n"
    "buffer_load_dwordx4 v3, s[96:99], s80 offen  lds\n"
    "s_add_u32 m0, s62, 20480\n"
    "s_add_u32 s80, s80, 49152\n"
    "buffer_load_dwordx4 v3, s[96:99], s80 offen  lds\n"
    "s_add_u32 m0, s62, 21504\n"
    "s_add_u32 s80, s80, 16384\n"
    "buffer_load_dwordx4 v3, s[96:99], s80 offen  lds\n"
    "s_add_u32 m0, s62, 22528\n"
    "s_add_u32 s80, s80, 49152\n"
    "buffer_load_dwordx4 v3, s[96:99], s80 offen  lds\n"
    "s_add_u32 m0, s62, 23552\n"
    "s_add_u32 s80, s80, 16384\n"
    "buffer_load_dwordx4 v3, s[96:99], s80 offen  lds\n"
    "s_waitcnt vmcnt(12)\n"
    "s_barrier\n"
    "s_lshl_b32 s64, s54, 6\n"
    "s_lshl_b32 s65, s55, 2\n"
    "s_add_u32 s64, s64, s65\n"
    "s_add_u32 s64, s64, s88\n"
    "s_lshl_b32 s64, s64, 7\n"
    "s_lshl_b32 s65, s89, 3\n"
    "s_add_u32 s64, s64, s65\n"
    "s_add_u32 s64, s64, 20480\n"
    "s_cmp_eq_u32 s54, 0\n"
    "s_cbranch_scc0 L_nf2_%=\n"
    "v_lshlrev_b32 v22, 12, v1\n"
    "v_add_u32 v22, s64, v22\n"
    "s_mov_b64 exec, 255\n"
    "global_store_dwordx2 v22, v[20:21], s[48:49] sc1\n"
    "s_mov_b64 exec, -1\n"
    "L_nf2_%=:\n"
    "s_add_u32 m0, s62, 24576\n"
    "s_add_u32 s79, s79, 1\n"
    "s_cmp_eq_u32 s79, 7\n"
    "s_cselect_b32 s79, 0, s79\n"
    "s_mul_i32 s80, s79, 262144\n"
    "s_add_u32 s80, s80, s78\n"
    "buffer_load_dwordx4 v3, s[96:99], s80 offen  lds\n"
    "s_add_u32 m0, s62, 25600\n"
    "s_add_u32 s80, s80, 16384\n"
    "buffer_load_dwordx4 v3, s[96:99], s80 offen  lds\n"
    "s_add_u32 m0, s62, 26624\n"
    "s_add_u32 s80, s80, 49152\n"
    "buffer_load_dwordx4 v3, s[96:99], s80 offen  lds\n"
    "s_add_u32 m0, s62, 27648\n"
    "s_add_u32 s80, s80, 16384\n"
    "buffer_load_dwordx4 v3, s[96:99], s80 offen  lds\n"
    "v_lshlrev_b32 v22, 16, v216\n"
    "v_mov_b32 v32, v22\n"
    "v_mov_b32 v33, v22\n"
    "v_mov_b32 v34, v22\n"
    "v_mov_b32 v35, v22\n"
    "v_mov_b32 v36, v22\n"
    "v_mov_b32 v37, v22\n"
    "v_mov_b32 v38, v22\n"
    "v_mov_b32 v39, v22\n"
    "v_mov_b32 v40, v22\n"
    "v_mov_b32 v41, v22\n"
    "v_mov_b32 v42, v22\n"
    "v_mov_b32 v43, v22\n"
    "v_mov_b32 v44, v22\n"
    "v_mov_b32 v45, v22\n"
    "v_mov_b32 v46, v22\n"
    "v_mov_b32 v47, v22\n"
    "ds_read_b64_tr_b16 a[0:1], v4 offset:0\n"
    "ds_read_b64_tr_b16 a[2:3], v4 offset:512\n"
    "ds_read_b64_tr_b16 a[4:5], v5 offset:0\n"
    "ds_read_b64_tr_b16 a[6:7], v5 offset:512\n"
    "ds_read_b64_tr_b16 a[8:9], v4 offset:2048\n"
    "ds_read_b64_tr_b16 a[10:11], v4 offset:2560\n"
    "ds_read_b64_tr_b16 a[12:13], v5 offset:2048\n"
    "ds_read_b64_tr_b16 a[14:15], v5 offset:2560\n"
    "s_waitcnt lgkmcnt(0)\n"
    "s_add_u32 m0, s62, 0\n"
    "s_add_u32 s80, s80, 49152\n"
    "buffer_load_dwordx4 v3, s[96:99], s80 offen  lds\n"
    "s_add_u32 m0, s62, 1024\n"
    "s_add_u32 s80, s80, 16384\n"
    "buffer_load_dwordx4 v3, s[96:99], s80 offen  lds\n"
    "s_add_u32 m0, s62, 2048\n"
    "s_add_u32 s80, s80, 49152\n"
    "buffer_load_dwordx4 v3, s[96:99], s80 offen  lds\n"
    "s_add_u32 m0, s62, 3072\n"
    "s_add_u32 s80, s80, 16384\n"
    "buffer_load_dwordx4 v3, s[96:99], s80 offen  lds\n"
    "ds_read_b64_tr_b16 a[16:17], v4 offset:4096\n"
    "ds_read_b64_tr_b16 a[18:19], v4 offset:4608\n"
    "ds_read_b64_tr_b16 a[20:21], v5 offset:4096\n"
    "ds_read_b64_tr_b16 a[22:23], v5 offset:4608\n"
    "ds_read_b64_tr_b16 a[24:25], v4 offset:6144\n"
    "ds_read_b64_tr_b16 a[26:27], v4 offset:6656\n"
    "ds_read_b64_tr_b16 a[28:29], v5 offset:6144\n"
    "ds_read_b64_tr_b16 a[30:31], v5 offset:6656\n"
    "s_waitcnt lgkmcnt(0)\n"
    "s_add_u32 m0, s62, 4096\n"
    "s_add_u32 s79, s79, 1\n"
    "s_cmp_eq_u32 s79, 7\n"
    "s_cselect_b32 s79, 0, s79\n"
    "s_mul_i32 s80, s79, 262144\n"
    "s_add_u32 s80, s80, s78\n"
    "buffer_load_dwordx4 v3, s[96:99], s80 offen  lds\n"
    "s_add_u32 m0, s62, 5120\n"
    "s_add_u32 s80, s80, 16384\n"
    "buffer_load_dwordx4 v3, s[96:99], s80 offen  lds\n"
    "s_add_u32 m0, s62, 6144\n"
    "s_add_u32 s80, s80, 49152\n"
    "buffer_load_dwordx4 v3, s[96:99], s80 offen  lds\n"
    "s_add_u32 m0, s62, 7168\n"
    "s_add_u32 s80, s80, 16384\n"
    "buffer_load_dwordx4 v3, s[96:99], s80 offen  lds\n"
    "ds_read_b64_tr_b16 a[32:33], v4 offset:8192\n"
    "ds_read_b64_tr_b16 a[34:35], v4 offset:8704\n"
    "ds_read_b64_tr_b16 a[36:37], v5 offset:8192\n"
    "ds_read_b64_tr_b16 a[38:39], v5 offset:8704\n"
    "ds_read_b64_tr_b16 a[40:41], v4 offset:10240\n"
    "ds_read_b64_tr_b16 a[42:43], v4 offset:10752\n"
    "ds_read_b64_tr_b16 a[44:45], v5 offset:10240\n"
    "ds_read_b64_tr_b16 a[46:47], v5 offset:10752\n"
    "s_waitcnt lgkmcnt(0)\n"
    "s_add_u32 m0, s62, 8192\n"
    "s_add_u32 s80, s80, 49152\n"
    "buffer_load_dwordx4 v3, s[96:99], s80 offen  lds\n"
    "s_add_u32 m0, s62, 9216\n"
    "s_add_u32 s80, s80, 16384\n"
    "buffer_load_dwordx4 v3, s[96:99], s80 offen  lds\n"
    "s_add_u32 m0, s62, 10240\n"
    "s_add_u32 s80, s80, 49152\n"
    "buffer_load_dwordx4 v3, s[96:99], s80 offen  lds\n"
    "s_add_u32 m0, s62, 11264\n"
    "s_add_u32 s80, s80, 16384\n"
    "buffer_load_dwordx4 v3, s[96:99], s80 offen  lds\n"
    "s_waitcnt vmcnt(26)\n"
    "ds_read_b64_tr_b16 a[48:49], v4 offset:12288\n"
    "ds_read_b64_tr_b16 a[50:51], v4 offset:12800\n"
    "ds_read_b64_tr_b16 a[52:53], v5 offset:12288\n"
    "ds_read_b64_tr_b16 a[54:55], v5 offset:12800\n"
    "s_waitcnt vmcnt(24)\n"
    "ds_read_b64_tr_b16 a[56:57], v4 offset:14336\n"
    "ds_read_b64_tr_b16 a[58:59], v4 offset:14848\n"
    "ds_read_b64_tr_b16 a[60:61], v5 offset:14336\n"
    "ds_read_b64_tr_b16 a[62:63], v5 offset:14848\n"
    "s_waitcnt lgkmcnt(0)\n"
    "s_add_u32 m0, s62, 12288\n"
    "s_add_u32 s79, s79, 1\n"
    "s_cmp_eq_u32 s79, 7\n"
    "s_cselect_b32 s79, 0, s79\n"
    "s_mul_i32 s80, s79, 262144\n"
    "s_add_u32 s80, s80, s78\n"
    "buffer_load_dwordx4 v3, s[96:99], s80 offen  lds\n"
    "s_add_u32 m0, s62, 13312\n"
    "s_add_u32 s80, s80, 16384\n"
    "buffer_load_dwordx4 v3, s[96:99], s80 offen  lds\n"
    "s_add_u32 m0, s62, 14336\n"
    "s_add_u32 s80, s80, 49152\n"
    "buffer_load_dwordx4 v3, s[96:99], s80 offen  lds\n"
    "s_add_u32 m0, s62, 15360\n"
    "s_add_u32 s80, s80, 16384\n"
    "buffer_load_dwordx4 v3, s[96:99], s80 offen  lds\n"
    "s_waitcnt vmcnt(26)\n"
    "ds_read_b64_tr_b16 a[64:65], v4 offset:16384\n"
    "ds_read_b64_tr_b16 a[66:67], v4 offset:16896\n"
    "ds_read_b64_tr_b16 a[68:69], v5 offset:16384\n"
    "ds_read_b64_tr_b16 a[70:71], v5 offset:16896\n"
    "s_waitcnt vmcnt(24)\n"
    "ds_read_b64_tr_b16 a[72:73], v4 offset:18432\n"
    "ds_read_b64_tr_b16 a[74:75], v4 offset:18944\n"
    "ds_read_b64_tr_b16 a[76:77], v5 offset:18432\n"
    "ds_read_b64_tr_b16 a[78:79], v5 offset:18944\n"
    "s_waitcnt lgkmcnt(0)\n"
    "s_add_u32 m0, s62, 16384\n"
    "s_add_u32 s80, s80, 49152\n"
    "buffer_load_dwordx4 v3, s[96:99], s80 offen  lds\n"
    "s_add_u32 m0, s62, 17408\n"
    "s_add_u32 s80, s80, 16384\n"
    "buffer_load_dwordx4 v3, s[96:99], s80 offen  lds\n"
    "s_add_u32 m0, s62, 18432\n"
    "s_add_u32 s80, s80, 49152\n"
    "buffer_load_dwordx4 v3, s[96:99], s80 offen  lds\n"
    "s_add_u32 m0, s62, 19456\n"
    "s_add_u32 s80, s80, 16384\n"
    "buffer_load_dwordx4 v3, s[96:99], s80 offen  lds\n"
    "s_waitcnt vmcnt(26)\n"
    "ds_read_b64_tr_b16 a[80:81], v4 offset:20480\n"
    "ds_read_b64_tr_b16 a[82:83], v4 offset:20992\n"
    "ds_read_b64_tr_b16 a[84:85], v5 offset:20480\n"
    "ds_read_b64_tr_b16 a[86:87], v5 offset:20992\n"
    "s_waitcnt vmcnt(24)\n"
    "ds_read_b64_tr_b16 a[88:89], v4 offset:22528\n"
    "ds_read_b64_tr_b16 a[90:91], v4 offset:23040\n"
    "ds_read_b64_tr_b16 a[92:93], v5 offset:22528\n"
    "ds_read_b64_tr_b16 a[94:95], v5 offset:23040\n"
    "s_waitcnt lgkmcnt(0)\n"
    "s_add_u32 m0, s62, 20480\n"
    "s_add_u32 s79, s79, 1\n"
    "s_cmp_eq_u32 s79, 7\n"
    "s_cselect_b32 s79, 0, s79\n"
    "s_mul_i32 s80, s79, 262144\n"
    "s_add_u32 s80, s80, s78\n"
    "buffer_load_dwordx4 v3, s[96:99], s80 offen  lds\n"
    "s_add_u32 m0, s62, 21504\n"
    "s_add_u32 s80, s80, 16384\n"
    "buffer_load_dwordx4 v3, s[96:99], s80 offen  lds\n"
    "s_add_u32 m0, s62, 22528\n"
    "s_add_u32 s80, s80, 49152\n"
    "buffer_load_dwordx4 v3, s[96:99], s80 offen  lds\n"
    "s_add_u32 m0, s62, 23552\n"
    "s_add_u32 s80, s80, 16384\n"
    "buffer_load_dwordx4 v3, s[96:99], s80 offen  lds\n"
    "global_load_dwordx2 v[224:225], v15, s[48:49] sc1\n"
    "s_waitcnt vmcnt(27)\n"
    "ds_read_b64_tr_b16 a[96:97], v4 offset:24576\n"
    "ds_read_b64_tr_b16 a[98:99], v4 offset:25088\n"
    "ds_read_b64_tr_b16 a[100:101], v5 offset:24576\n"
    "ds_read_b64_tr_b16 a[102:103], v5 offset:25088\n"
    "s_waitcnt vmcnt(25)\n"
    "ds_read_b64_tr_b16 a[104:105], v4 offset:26624\n"
    "ds_read_b64_tr_b16 a[106:107], v4 offset:27136\n"
    "ds_read_b64_tr_b16 a[108:109], v5 offset:26624\n"
    "ds_read_b64_tr_b16 a[110:111], v5 offset:27136\n"
    "s_waitcnt lgkmcnt(0)\n"
    "s_add_u32 m0, s62, 24576\n"
    "s_add_u32 s80, s80, 49152\n"
    "buffer_load_dwordx4 v3, s[96:99], s80 offen  lds\n"
    "s_add_u32 m0, s62, 25600\n"
    "s_add_u32 s80, s80, 16384\n"
    "buffer_load_dwordx4 v3, s[96:99], s80 offen  lds\n"
    "s_add_u32 m0, s62, 26624\n"
    "s_add_u32 s80, s80, 49152\n"
    "buffer_load_dwordx4 v3, s[96:99], s80 offen  lds\n"
    "s_add_u32 m0, s62, 27648\n"
    "s_add_u32 s80, s80, 16384\n"
    "buffer_load_dwordx4 v3, s[96:99], s80 offen  lds\n"
    "s_mul_i32 s72, s91, 229376\n"
    "s_mul_i32 s64, s54, 57344\n"
    "s_add_u32 s72, s72, s64\n"
    "s_mov_b32 s73, s82\n"
    "s_waitcnt vmcnt(4)\n"
    "s_mov_b32 s71, 0\n"
    "L_p2_%=:\n"
    "v_cmp_ne_u64 s[64:65], s[50:51], v[224:225]\n"
    "s_nop 1\n"
    "s_cmp_eq_u64 s[64:65], 0\n"
    "s_cbranch_scc1 L_p2_done_%=\n"
    "s_sleep 1\n"
    "global_load_dwordx2 v[224:225], v15, s[48:49] sc1\n"
    "s_add_u32 s71, s71, 1\n"
    "s_cmp_lt_u32 s71, 400000\n"
    "s_waitcnt vmcnt(0)\n"
    "s_cbranch_scc1 L_p2_%=\n"
    "L_p2_done_%=:\n"
    "s_lshl_b32 s63, s73, 13\n"
    "s_add_u32 s63, s63, s72\n"
    "buffer_load_dwordx4 v[96:99], v6, s[40:43], s63 offen offset:0 sc1\n"
    "ds_read_b64_tr_b16 a[112:113], v4 offset:0\n"
    "ds_read_b64_tr_b16 a[114:115], v4 offset:512\n"
    "ds_read_b64_tr_b16 a[116:117], v5 offset:0\n"
    "ds_read_b64_tr_b16 a[118:119], v5 offset:512\n"
    "buffer_load_dwordx4 v[100:103], v7, s[40:43], s63 offen offset:0 sc1\n"
    "ds_read_b64_tr_b16 a[120:121], v4 offset:2048\n"
    "ds_read_b64_tr_b16 a[122:123], v4 offset:2560\n"
    "ds_read_b64_tr_b16 a[124:125], v5 offset:2048\n"
    "ds_read_b64_tr_b16 a[126:127], v5 offset:2560\n"
    "buffer_load_dwordx4 v[104:107], v6, s[40:43], s63 offen offset:2048 sc1\n"
    "ds_read_b64_tr_b16 a[128:129], v4 offset:4096\n"
    "ds_read_b64_tr_b16 a[130:131], v4 offset:4608\n"
    "ds_read_b64_tr_b16 a[132:133], v5 offset:4096\n"
    "ds_read_b64_tr_b16 a[134:135], v5 offset:4608\n"
    "buffer_load_dwordx4 v[108:111], v7, s[40:43], s63 offen offset:2048 sc1\n"
    "ds_read_b64_tr_b16 a[136:137], v4 offset:6144\n"
    "ds_read_b64_tr_b16 a[138:139], v4 offset:6656\n"
    "ds_read_b64_tr_b16 a[140:141], v5 offset:6144\n"
    "ds_read_b64_tr_b16 a[142:143], v5 offset:6656\n"
    "s_add_u32 s63, s63, 4096\n"
    "buffer_load_dwordx4 v[112:115], v6, s[40:43], s63 offen offset:0 sc1\n"
    "ds_read_b64_tr_b16 a[144:145], v4 offset:8192\n"
    "ds_read_b64_tr_b16 a[146:147], v4 offset:8704\n"
    "ds_read_b64_tr_b16 a[148:149], v5 offset:8192\n"
    "ds_read_b64_tr_b16 a[150:151], v5 offset:8704\n"
    "buffer_load_dwordx4 v[116:119], v7, s[40:43], s63 offen offset:0 sc1\n"
    "ds_read_b64_tr_b16 a[152:153], v4 offset:10240\n"
    "ds_read_b64_tr_b16 a[154:155], v4 offset:10752\n"
    "ds_read_b64_tr_b16 a[156:157], v5 offset:10240\n"
    "ds_read_b64_tr_b16 a[158:159], v5 offset:10752\n"
    "buffer_load_dwordx4 v[120:123], v6, s[40:43], s63 offen offset:2048 sc1\n"
    "ds_read_b64_tr_b16 a[160:161], v4 offset:12288\n"
    "ds_read_b64_tr_b16 a[162:163], v4 offset:12800\n"
    "ds_read_b64_tr_b16 a[164:165], v5 offset:12288\n"
    "ds_read_b64_tr_b16 a[166:167], v5 offset:12800\n"
    "buffer_load_dwordx4 v[124:127], v7, s[40:43], s63 offen offset:2048 sc1\n"
    "ds_read_b64_tr_b16 a[168:169], v4 offset:14336\n"
    "ds_read_b64_tr_b16 a[170:171], v4 offset:14848\n"
    "ds_read_b64_tr_b16 a[172:173], v5 offset:14336\n"
    "ds_read_b64_tr_b16 a[174:175], v5 offset:14848\n"
    "s_add_u32 s73, s73, 1\n"
    "s_cmp_eq_u32 s73, 7\n"
    "s_cselect_b32 s73, 0, s73\n"
    "s_lshl_b32 s63, s73, 13\n"
    "s_add_u32 s63, s63, s72\n"
    "buffer_load_dwordx4 v[128:131], v6, s[40:43], s63 offen offset:0 sc1\n"
    "ds_read_b64_tr_b16 a[176:177], v4 offset:16384\n"
    "ds_read_b64_tr_b16 a[178:179], v4 offset:16896\n"
    "ds_read_b64_tr_b16 a[180:181], v5 offset:16384\n"
    "ds_read_b64_tr_b16 a[182:183], v5 offset:16896\n"
    "buffer_load_dwordx4 v[132:135], v7, s[40:43], s63 offen offset:0 sc1\n"
    "ds_read_b64_tr_b16 a[184:185], v4 offset:18432\n"
    "ds_read_b64_tr_b16 a[186:187], v4 offset:18944\n"
    "ds_read_b64_tr_b16 a[188:189], v5 offset:18432\n"
    "ds_read_b64_tr_b16 a[190:191], v5 offset:18944\n"
    "ds_read_b64_tr_b16 a[192:193], v4 offset:20480\n"
    "ds_read_b64_tr_b16 a[194:195], v4 offset:20992\n"
    "ds_read_b64_tr_b16 a[196:197], v5 offset:20480\n"
    "ds_read_b64_tr_b16 a[198:199], v5 offset:20992\n"
    "ds_read_b64_tr_b16 a[200:201], v4 offset:22528\n"
    "ds_read_b64_tr_b16 a[202:203], v4 offset:23040\n"
    "ds_read_b64_tr_b16 a[204:205], v5 offset:22528\n"
    "ds_read_b64_tr_b16 a[206:207], v5 offset:23040\n"
    "s_waitcnt vmcnt(12)\n"
    "ds_read_b64_tr_b16 a[208:209], v4 offset:24576\n"
    "ds_read_b64_tr_b16 a[210:211], v4 offset:25088\n"
    "ds_read_b64_tr_b16 a[212:213], v5 offset:24576\n"
    "ds_read_b64_tr_b16 a[214:215], v5 offset:25088\n"
    "s_waitcnt vmcnt(10)\n"
    "ds_read_b64_tr_b16 a[216:217], v4 offset:26624\n"
    "ds_read_b64_tr_b16 a[218:219], v4 offset:27136\n"
    "ds_read_b64_tr_b16 a[220:221], v5 offset:26624\n"
    "ds_read_b64_tr_b16 a[222:223], v5 offset:27136\n"
    "s_waitcnt lgkmcnt(0)\n"
    "s_branch L_gemm2_%=\n"
    "L_idle_%=:\n"
    "v_mul_u32_u24 v26, 28672, v0\n"
    "buffer_load_dword v230, v26, s[96:99], 0 offen\n"
    "v_lshlrev_b32 v26, 13, v0\n"
    "buffer_load_dword v231, v26, s[44:47], 0 offen\n"
    "v_mul_u32_u24 v26, 28672, v0\n"
    "buffer_load_dword v232, v26, s[28:31], 0 offen\n"
    "v_mov_b32 v30, 0\n"
    "s_mov_b64 exec, 1\n"
    "global_load_dword v233, v30, s[48:49] offset:-4096\n"
    "global_load_dword v234, v30, s[36:37] offset:-4096\n"
    "s_mov_b64 exec, -1\n"
    "v_lshrrev_b32 v22, 5, v1\n"
    "v_bfe_u32 v23, v1, 4, 1\n"
    "v_bfe_u32 v24, v1, 2, 2\n"
    "v_and_b32 v25, 3, v1\n"
    "v_lshl_add_u32 v26, v22, 3, v24\n"
    "v_lshlrev_b32 v26, 7, v26\n"
    "v_lshrrev_b32 v27, 1, v25\n"
    "v_lshl_add_u32 v27, v23, 1, v27\n"
    "v_lshrrev_b32 v28, 1, v24\n"
    "s_lshr_b32 s66, s54, 1\n"
    "v_xor_b32 v28, s66, v28\n"
    "v_lshl_add_u32 v27, v28, 2, v27\n"
    "v_lshl_add_u32 v26, v27, 4, v26\n"
    "v_and_b32 v27, 1, v25\n"
    "v_lshl_add_u32 v26, v27, 3, v26\n"
    "v_add_u32 v4, s62, v26\n"
    "v_xor_b32 v5, 64, v4\n"
    "v_mul_u32_u24 v26, 1088, v24\n"
    "v_lshlrev_b32 v27, 6, v22\n"
    "v_lshl_add_u32 v27, v23, 5, v27\n"
    "v_lshl_add_u32 v27, v25, 3, v27\n"
    "v_add_u32 v26, v26, v27\n"
    "s_lshl_b32 s67, s54, 8\n"
    "s_add_u32 s67, s67, 114688\n"
    "v_add_u32 v17, s67, v26\n"
    "s_mul_i32 s67, s54, 2176\n"
    "s_add_u32 s67, s67, 114688\n"
    "v_add_u32 v16, s67, v12\n"
    "v_and_b32 v26, 31, v1\n"
    "v_lshlrev_b32 v26, 4, v26\n"
    "v_lshl_add_u32 v18, v22, 10, v26\n"
    "v_and_b32 v26, 31, v1\n"
    "v_lshlrev_b32 v26, 10, v26\n"
    "v_lshl_add_u32 v13, v22, 4, v26\n"
    "s_and_b32 s67, s54, 1\n"
    "s_lshl_b32 s67, s67, 10\n"
    "v_add_u32 v6, s67, v12\n"
    "v_xor_b32 v7, 0x400, v6\n"
    "s_xor_b32 s67, s54, 1\n"
    "s_mul_i32 s67, s67, 12288\n"
    "s_add_u32 s67, s67, 114688\n"
    "v_add_u32 v8, s67, v12\n"
    "s_xor_b32 s67, s54, 2\n"
    "s_mul_i32 s67, s67, 12288\n"
    "s_add_u32 s67, s67, 118784\n"
    "v_add_u32 v9, s67, v12\n"
    "s_xor_b32 s67, s54, 3\n"
    "s_mul_i32 s67, s67, 12288\n"
    "s_add_u32 s67, s67, 122880\n"
    "v_add_u32 v10, s67, v12\n"
    "s_mul_i32 s67, s54, 12288\n"
    "s_add_u32 s67, s67, 114688\n"
    "v_add_u32 v11, s67, v12\n"
    "s_and_b32 s68, s70, 7\n"
    "s_lshl_b32 s68, s68, 11\n"
    "s_lshl_b32 s67, s54, 9\n"
    "s_add_u32 s67, s67, s68\n"
    "s_add_u32 s67, s67, 4096\n"
    "v_lshlrev_b32 v26, 3, v1\n"
    "v_add_u32 v14, s67, v26\n"
    "s_lshl_b32 s68, s90, 5\n"
    "s_lshl_b32 s67, s91, 2\n"
    "s_add_u32 s67, s67, s68\n"
    "s_add_u32 s67, s67, s54\n"
    "s_lshl_b32 s67, s67, 7\n"
    "s_add_u32 s67, s67, 20480\n"
    "v_min_u32 v26, 6, v1\n"
    "v_lshlrev_b32 v26, 3, v26\n"
    "v_add_u32 v15, s67, v26\n"
    "v_mov_b32 v218, 0xc0135761\n"
    "v_mov_b32 v219, 0xc0135761\n"
    "v_mov_b32 v220, 0xbdd2d3e8\n"
    "v_mov_b32 v221, 0xbdd2d3e8\n"
    "v_mov_b32 v222, 1.0\n"
    "v_mov_b32 v223, 1.0\n"
    "s_waitcnt vmcnt(7)\n"
    "s_mov_b32 s69, 0x3b000000\n"
    "v_lshlrev_b32 v96, 16, v240\n"
    "v_and_b32 v97, 0xffff0000, v240\n"
    "v_lshlrev_b32 v98, 16, v241\n"
    "v_and_b32 v99, 0xffff0000, v241\n"
    "v_lshlrev_b32 v100, 16, v242\n"
    "v_and_b32 v101, 0xffff0000, v242\n"
    "v_lshlrev_b32 v102, 16, v243\n"
    "v_and_b32 v103, 0xffff0000, v243\n"
    "v_lshlrev_b32 v104, 16, v244\n"
    "v_and_b32 v105, 0xffff0000, v244\n"
    "v_lshlrev_b32 v106, 16, v245\n"
    "v_and_b32 v107, 0xffff0000, v245\n"
    "v_lshlrev_b32 v108, 16, v246\n"
    "v_and_b32 v109, 0xffff0000, v246\n"
    "v_lshlrev_b32 v110, 16, v247\n"
    "v_and_b32 v111, 0xffff0000, v247\n"
    "v_pk_add_f32 v[112:113], v[96:97], v[98:99]\n"
    "v_pk_add_f32 v[114:115], v[100:101], v[102:103]\n"
    "v_pk_add_f32 v[116:117], v[104:105], v[106:107]\n"
    "v_pk_add_f32 v[118:119], v[108:109], v[110:111]\n"
    "v_pk_add_f32 v[112:113], v[112:113], v[114:115]\n"
    "v_pk_add_f32 v[116:117], v[116:117], v[118:119]\n"
    "v_add_f32 v112, v112, v113\n"
    "v_add_f32 v116, v116, v117\n"
    "s_nop 0\n"
    "v_add_f32_dpp v112, v112, v112 row_shr:1 row_mask:0xf bank_mask:0xf\n"
    "v_add_f32_dpp v116, v116, v116 row_shr:1 row_mask:0xf bank_mask:0xf\n"
    "s_nop 0\n"
    "v_add_f32_dpp v112, v112, v112 row_shr:2 row_mask:0xf bank_mask:0xf\n"
    "v_add_f32_dpp v116, v116, v116 row_shr:2 row_mask:0xf bank_mask:0xf\n"
    "s_nop 0\n"
    "v_add_f32_dpp v112, v112, v112 row_shr:4 row_mask:0xf bank_mask:0xf\n"
    "v_add_f32_dpp v116, v116, v116 row_shr:4 row_mask:0xf bank_mask:0xf\n"
    "s_nop 0\n"
    "v_add_f32_dpp v112, v112, v112 row_shr:8 row_mask:0xf bank_mask:0xf\n"
    "v_add_f32_dpp v116, v116, v116 row_shr:8 row_mask:0xf bank_mask:0xf\n"
    "s_nop 0\n"
    "v_add_f32_dpp v112, v112, v112 row_bcast:15 row_mask:0xa bank_mask:0xf\n"
    "v_add_f32_dpp v116, v116, v116 row_bcast:15 row_mask:0xa bank_mask:0xf\n"
    "s_nop 0\n"
    "v_add_f32_dpp v112, v112, v112 row_bcast:31 row_mask:0xc bank_mask:0xf\n"
    "v_add_f32_dpp v116, v116, v116 row_bcast:31 row_mask:0xc bank_mask:0xf\n"
    "v_mul_f32 v112, 0x3b000000, v112\n"
    "v_mul_f32 v116, 0x3b000000, v116\n"
    "v_cvt_pk_bf16_f32 v120, v112, v112\n"
    "v_cvt_pk_bf16_f32 v121, v116, v116\n"
    "v_and_b32 v120, 0xffff0000, v120\n"
    "v_and_b32 v121, 0xffff0000, v121\n"
    "v_readlane_b32 s64, v120, 63\n"
    "v_readlane_b32 s66, v121, 63\n"
    "v_sub_f32 v112, v112, v120\n"
    "v_sub_f32 v116, v116, v121\n"
    "v_pk_add_f32 v[96:97], v[96:97], s[64:65] op_sel_hi:[1,0] neg_lo:[0,1] neg_hi:[0,1]\n"
    "v_pk_add_f32 v[98:99], v[98:99], s[64:65] op_sel_hi:[1,0] neg_lo:[0,1] neg_hi:[0,1]\n"
    "v_pk_add_f32 v[100:101], v[100:101], s[64:65] op_sel_hi:[1,0] neg_lo:[0,1] neg_hi:[0,1]\n"
    "v_pk_add_f32 v[102:103], v[102:103], s[64:65] op_sel_hi:[1,0] neg_lo:[0,1] neg_hi:[0,1]\n"
    "v_pk_add_f32 v[104:105], v[104:105], s[66:67] op_sel_hi:[1,0] neg_lo:[0,1] neg_hi:[0,1]\n"
    "v_pk_add_f32 v[106:107], v[106:107], s[66:67] op_sel_hi:[1,0] neg_lo:[0,1] neg_hi:[0,1]\n"
    "v_pk_add_f32 v[108:109], v[108:109], s[66:67] op_sel_hi:[1,0] neg_lo:[0,1] neg_hi:[0,1]\n"
    "v_pk_add_f32 v[110:111], v[110:111], s[66:67] op_sel_hi:[1,0] neg_lo:[0,1] neg_hi:[0,1]\n"
    "v_pk_mul_f32 v[122:123], v[96:97], v[96:97]\n"
    "v_pk_mul_f32 v[124:125], v[104:105], v[104:105]\n"
    "v_pk_fma_f32 v[122:123], v[98:99], v[98:99], v[122:123]\n"
    "v_pk_fma_f32 v[124:125], v[106:107], v[106:107], v[124:125]\n"
    "v_pk_fma_f32 v[122:123], v[100:101], v[100:101], v[122:123]\n"
    "v_pk_fma_f32 v[124:125], v[108:109], v[108:109], v[124:125]\n"
    "v_pk_fma_f32 v[122:123], v[102:103], v[102:103], v[122:123]\n"
    "v_pk_fma_f32 v[124:125], v[110:111], v[110:111], v[124:125]\n"
    "v_add_f32 v122, v122, v123\n"
    "v_add_f32 v124, v124, v125\n"
    "s_nop 0\n"
    "v_add_f32_dpp v122, v122, v122 row_shr:1 row_mask:0xf bank_mask:0xf\n"
    "v_add_f32_dpp v124, v124, v124 row_shr:1 row_mask:0xf bank_mask:0xf\n"
    "s_nop 0\n"
    "v_add_f32_dpp v122, v122, v122 row_shr:2 row_mask:0xf bank_mask:0xf\n"
    "v_add_f32_dpp v124, v124, v124 row_shr:2 row_mask:0xf bank_mask:0xf\n"
    "s_nop 0\n"
    "v_add_f32_dpp v122, v122, v122 row_shr:4 row_mask:0xf bank_mask:0xf\n"
    "v_add_f32_dpp v124, v124, v124 row_shr:4 row_mask:0xf bank_mask:0xf\n"
    "s_nop 0\n"
    "v_add_f32_dpp v122, v122, v122 row_shr:8 row_mask:0xf bank_mask:0xf\n"
    "v_add_f32_dpp v124, v124, v124 row_shr:8 row_mask:0xf bank_mask:0xf\n"
    "s_nop 0\n"
    "v_add_f32_dpp v122, v122, v122 row_bcast:15 row_mask:0xa bank_mask:0xf\n"
    "v_add_f32_dpp v124, v124, v124 row_bcast:15 row_mask:0xa bank_mask:0xf\n"
    "s_nop 0\n"
    "v_add_f32_dpp v122, v122, v122 row_bcast:31 row_mask:0xc bank_mask:0xf\n"
    "v_add_f32_dpp v124, v124, v124 row_bcast:31 row_mask:0xc bank_mask:0xf\n"
    "v_mul_f32 v112, v112, v112\n"
    "v_mul_f32 v116, v116, v116\n"
    "v_fma_f32 v122, v122, s69, -v112\n"
    "v_fma_f32 v124, v124, s69, -v116\n"
    "v_max_f32 v122, 0, v122\n"
    "v_max_f32 v124, 0, v124\n"
    "v_cvt_pk_bf16_f32 v122, v122, v122\n"
    "v_cvt_pk_bf16_f32 v124, v124, v124\n"
    "v_and_b32 v122, 0xffff0000, v122\n"
    "v_and_b32 v124, 0xffff0000, v124\n"
    "v_add_f32 v122, 0x3727c5ac, v122\n"
    "v_add_f32 v124, 0x3727c5ac, v124\n"
    "v_cvt_pk_bf16_f32 v122, v122, v122\n"
    "v_cvt_pk_bf16_f32 v124, v124, v124\n"
    "v_and_b32 v122, 0xffff0000, v122\n"
    "v_and_b32 v124, 0xffff0000, v124\n"
    "v_sqrt_f32 v122, v122\n"
    "v_sqrt_f32 v124, v124\n"
    "v_cvt_pk_bf16_f32 v122, v122, v122\n"
    "v_cvt_pk_bf16_f32 v124, v124, v124\n"
    "v_and_b32 v122, 0xffff0000, v122\n"
    "v_and_b32 v124, 0xffff0000, v124\n"
    "v_rcp_f32 v122, v122\n"
    "v_rcp_f32 v124, v124\n"
    "v_readlane_b32 s64, v122, 63\n"
    "v_readlane_b32 s66, v124, 63\n"
    "s_nop 0\n"
    "v_pk_mul_f32 v[96:97], v[96:97], s[64:65] op_sel_hi:[1,0]\n"
    "v_pk_mul_f32 v[98:99], v[98:99], s[64:65] op_sel_hi:[1,0]\n"
    "v_pk_mul_f32 v[100:101], v[100:101], s[64:65] op_sel_hi:[1,0]\n"
    "v_pk_mul_f32 v[102:103], v[102:103], s[64:65] op_sel_hi:[1,0]\n"
    "v_pk_mul_f32 v[104:105], v[104:105], s[66:67] op_sel_hi:[1,0]\n"
    "v_pk_mul_f32 v[106:107], v[106:107], s[66:67] op_sel_hi:[1,0]\n"
    "v_pk_mul_f32 v[108:109], v[108:109], s[66:67] op_sel_hi:[1,0]\n"
    "v_pk_mul_f32 v[110:111], v[110:111], s[66:67] op_sel_hi:[1,0]\n"
    "v_cvt_pk_bf16_f32 v128, v96, v97\n"
    "v_cvt_pk_bf16_f32 v129, v98, v99\n"
    "v_cvt_pk_bf16_f32 v130, v100, v101\n"
    "v_cvt_pk_bf16_f32 v131, v102, v103\n"
    "v_cvt_pk_bf16_f32 v132, v104, v105\n"
    "v_cvt_pk_bf16_f32 v133, v106, v107\n"
    "v_cvt_pk_bf16_f32 v134, v108, v109\n"
    "v_cvt_pk_bf16_f32 v135, v110, v111\n"
    "ds_write_b128 v16, v[128:131]\n"
    "ds_write_b128 v16, v[132:135] offset:1088\n"
    "s_lshl_b32 s64, s54, 19\n"
    "s_lshr_b32 s65, s70, 1\n"
    "s_lshl_b32 s65, s65, 11\n"
    "s_add_u32 s64, s64, s65\n"
    "s_and_b32 s65, s70, 1\n"
    "s_lshl_b32 s65, s65, 9\n"
    "s_add_u32 s64, s64, s65\n"
    "s_add_u32 s65, s64, 0x40000\n"
    "s_waitcnt lgkmcnt(0)\n"
    "s_barrier\n"
    "ds_read_b64_tr_b16 v[136:137], v17 offset:0\n"
    "ds_read_b64_tr_b16 v[138:139], v17 offset:4352\n"
    "ds_read_b64_tr_b16 v[140:141], v17 offset:128\n"
    "ds_read_b64_tr_b16 v[142:143], v17 offset:4480\n"
    "s_waitcnt vmcnt(6)\n"
    "v_readfirstlane_b32 s52, v20\n"
    "v_readfirstlane_b32 s53, v21\n"
    "s_add_u32 s52, s52, 1\n"
    "s_addc_u32 s53, s53, 0\n"
    "s_mul_i32 s50, s52, 0x7f4a7c15\n"
    "s_mul_hi_u32 s51, s52, 0x7f4a7c15\n"
    "s_mul_i32 s66, s52, 0x9e3779b9\n"
    "s_add_u32 s51, s51, s66\n"
    "s_mul_i32 s66, s53, 0x7f4a7c15\n"
    "s_add_u32 s51, s51, s66\n"
    "v_mov_b32 v20, s50\n"
    "v_mov_b32 v21, s51\n"
    "s_waitcnt lgkmcnt(0)\n"
    "buffer_store_dwordx4 v[136:139], v18, s[36:39], s64 offen sc1\n"
    "buffer_store_dwordx4 v[140:143], v18, s[36:39], s65 offen sc1\n"
    "s_waitcnt vmcnt(0)\n"
    "s_barrier\n"
    "s_lshl_b32 s64, s70, 3\n"
    "s_lshl_b32 s65, s54, 12\n"
    "s_add_u32 s64, s64, s65\n"
    "s_add_u32 s64, s64, 4096\n"
    "s_cmp_eq_u32 s54, 0\n"
    "s_cbranch_scc0 L_nf3_%=\n"
    "v_lshlrev_b32 v22, 11, v1\n"
    "v_add_u32 v22, s64, v22\n"
    "s_mov_b64 exec, 255\n"
    "global_store_dwordx2 v22, v[20:21], s[48:49] sc1\n"
    "s_mov_b64 exec, -1\n"
    "L_nf3_%=:\n"
    "s_add_u32 m0, s62, 0\n"
    "s_mul_i32 s80, s79, 262144\n"
    "s_add_u32 s80, s80, s78\n"
    "buffer_load_dwordx4 v3, s[96:99], s80 offen  lds\n"
    "s_add_u32 m0, s62, 1024\n"
    "s_add_u32 s80, s80, 16384\n"
    "buffer_load_dwordx4 v3, s[96:99], s80 offen  lds\n"
    "s_add_u32 m0, s62, 2048\n"
    "s_add_u32 s80, s80, 49152\n"
    "buffer_load_dwordx4 v3, s[96:99], s80 offen  lds\n"
    "s_add_u32 m0, s62, 3072\n"
    "s_add_u32 s80, s80, 16384\n"
    "buffer_load_dwordx4 v3, s[96:99], s80 offen  lds\n"
    "s_add_u32 m0, s62, 4096\n"
    "s_add_u32 s80, s80, 49152\n"
    "buffer_load_dwordx4 v3, s[96:99], s80 offen  lds\n"
    "s_add_u32 m0, s62, 5120\n"
    "s_add_u32 s80, s80, 16384\n"
    "buffer_load_dwordx4 v3, s[96:99], s80 offen  lds\n"
    "s_add_u32 m0, s62, 6144\n"
    "s_add_u32 s80, s80, 49152\n"
    "buffer_load_dwordx4 v3, s[96:99], s80 offen  lds\n"
    "s_add_u32 m0, s62, 7168\n"
    "s_add_u32 s80, s80, 16384\n"
    "buffer_load_dwordx4 v3, s[96:99], s80 offen  lds\n"
    "s_add_u32 m0, s62, 8192\n"
    "s_add_u32 s79, s79, 1\n"
    "s_cmp_eq_u32 s79, 7\n"
    "s_cselect_b32 s79, 0, s79\n"
    "s_mul_i32 s80, s79, 262144\n"
    "s_add_u32 s80, s80, s78\n"
    "buffer_load_dwordx4 v3, s[96:99], s80 offen  lds\n"
    "s_add_u32 m0, s62, 9216\n"
    "s_add_u32 s80, s80, 16384\n"
    "buffer_load_dwordx4 v3, s[96:99], s80 offen  lds\n"
    "s_add_u32 m0, s62, 10240\n"
    "s_add_u32 s80, s80, 49152\n"
    "buffer_load_dwordx4 v3, s[96:99], s80 offen  lds\n"
    "s_add_u32 m0, s62, 11264\n"
    "s_add_u32 s80, s80, 16384\n"
    "buffer_load_dwordx4 v3, s[96:99], s80 offen  lds\n"
    "s_add_u32 m0, s62, 12288\n"
    "s_add_u32 s80, s80, 49152\n"
    "buffer_load_dwordx4 v3, s[96:99], s80 offen  lds\n"
    "s_add_u32 m0, s62, 13312\n"
    "s_add_u32 s80, s80, 16384\n"
    "buffer_load_dwordx4 v3, s[96:99], s80 offen  lds\n"
    "s_add_u32 m0, s62, 14336\n"
    "s_add_u32 s80, s80, 49152\n"
    "buffer_load_dwordx4 v3, s[96:99], s80 offen  lds\n"
    "s_add_u32 m0, s62, 15360\n"
    "s_add_u32 s80, s80, 16384\n"
    "buffer_load_dwordx4 v3, s[96:99], s80 offen  lds\n"
    "s_add_u32 m0, s62, 16384\n"
    "s_add_u32 s79, s79, 1\n"
    "s_cmp_eq_u32 s79, 7\n"
    "s_cselect_b32 s79, 0, s79\n"
    "s_mul_i32 s80, s79, 262144\n"
    "s_add_u32 s80, s80, s78\n"
    "buffer_load_dwordx4 v3, s[96:99], s80 offen  lds\n"
    "s_add_u32 m0, s62, 17408\n"
    "s_add_u32 s80, s80, 16384\n"
    "buffer_load_dwordx4 v3, s[96:99], s80 offen  lds\n"
    "s_add_u32 m0, s62, 18432\n"
    "s_add_u32 s80, s80, 49152\n"
    "buffer_load_dwordx4 v3, s[96:99], s80 offen  lds\n"
    "s_add_u32 m0, s62, 19456\n"
    "s_add_u32 s80, s80, 16384\n"
    "buffer_load_dwordx4 v3, s[96:99], s80 offen  lds\n"
    "s_add_u32 m0, s62, 20480\n"
    "s_add_u32 s80, s80, 49152\n"
    "buffer_load_dwordx4 v3, s[96:99], s80 offen  lds\n"
    "s_add_u32 m0, s62, 21504\n"
    "s_add_u32 s80, s80, 16384\n"
    "buffer_load_dwordx4 v3, s[96:99], s80 offen  lds\n"
    "s_add_u32 m0, s62, 22528\n"
    "s_add_u32 s80, s80, 49152\n"
    "buffer_load_dwordx4 v3, s[96:99], s80 offen  lds\n"
    "s_add_u32 m0, s62, 23552\n"
    "s_add_u32 s80, s80, 16384\n"
    "buffer_load_dwordx4 v3, s[96:99], s80 offen  lds\n"
    "s_add_u32 m0, s62, 24576\n"
    "s_add_u32 s79, s79, 1\n"
    "s_cmp_eq_u32 s79, 7\n"
    "s_cselect_b32 s79, 0, s79\n"
    "s_mul_i32 s80, s79, 262144\n"
    "s_add_u32 s80, s80, s78\n"
    "buffer_load_dwordx4 v3, s[96:99], s80 offen  lds\n"
    "s_add_u32 m0, s62, 25600\n"
    "s_add_u32 s80, s80, 16384\n"
    "buffer_load_dwordx4 v3, s[96:99], s80 offen  lds\n"
    "s_add_u32 m0, s62, 26624\n"
    "s_add_u32 s80, s80, 49152\n"
    "buffer_load_dwordx4 v3, s[96:99], s80 offen  lds\n"
    "s_add_u32 m0, s62, 27648\n"
    "s_add_u32 s80, s80, 16384\n"
    "buffer_load_dwordx4 v3, s[96:99], s80 offen  lds\n"
    "v_lshlrev_b32 v22, 16, v216\n"
    "v_mov_b32 v32, v22\n"
    "v_mov_b32 v33, v22\n"
    "v_mov_b32 v34, v22\n"
    "v_mov_b32 v35, v22\n"
    "v_mov_b32 v36, v22\n"
    "v_mov_b32 v37, v22\n"
    "v_mov_b32 v38, v22\n"
    "v_mov_b32 v39, v22\n"
    "v_mov_b32 v40, v22\n"
    "v_mov_b32 v41, v22\n"
    "v_mov_b32 v42, v22\n"
    "v_mov_b32 v43, v22\n"
    "v_mov_b32 v44, v22\n"
    "v_mov_b32 v45, v22\n"
    "v_mov_b32 v46, v22\n"
    "v_mov_b32 v47, v22\n"
    "s_waitcnt vmcnt(26)\n"
    "ds_read_b64_tr_b16 a[0:1], v4 offset:0\n"
    "ds_read_b64_tr_b16 a[2:3], v4 offset:512\n"
    "ds_read_b64_tr_b16 a[4:5], v5 offset:0\n"
    "ds_read_b64_tr_b16 a[6:7], v5 offset:512\n"
    "s_waitcnt vmcnt(24)\n"
    "ds_read_b64_tr_b16 a[8:9], v4 offset:2048\n"
    "ds_read_b64_tr_b16 a[10:11], v4 offset:2560\n"
    "ds_read_b64_tr_b16 a[12:13], v5 offset:2048\n"
    "ds_read_b64_tr_b16 a[14:15], v5 offset:2560\n"
    "s_waitcnt lgkmcnt(0)\n"
    "s_add_u32 m0, s62, 0\n"
    "s_add_u32 s80, s80, 49152\n"
    "buffer_load_dwordx4 v3, s[96:99], s80 offen  lds\n"
    "s_add_u32 m0, s62, 1024\n"
    "s_add_u32 s80, s80, 16384\n"
    "buffer_load_dwordx4 v3, s[96:99], s80 offen  lds\n"
    "s_add_u32 m0, s62, 2048\n"
    "s_add_u32 s80, s80, 49152\n"
    "buffer_load_dwordx4 v3, s[96:99], s80 offen  lds\n"
    "s_add_u32 m0, s62, 3072\n"
    "s_add_u32 s80, s80, 16384\n"
    "buffer_load_dwordx4 v3, s[96:99], s80 offen  lds\n"
    "s_waitcnt vmcnt(26)\n"
    "ds_read_b64_tr_b16 a[16:17], v4 offset:4096\n"
    "ds_read_b64_tr_b16 a[18:19], v4 offset:4608\n"
    "ds_read_b64_tr_b16 a[20:21], v5 offset:4096\n"
    "ds_read_b64_tr_b16 a[22:23], v5 offset:4608\n"
    "s_waitcnt vmcnt(24)\n"
    "ds_read_b64_tr_b16 a[24:25], v4 offset:6144\n"
    "ds_read_b64_tr_b16 a[26:27], v4 offset:6656\n"
    "ds_read_b64_tr_b16 a[28:29], v5 offset:6144\n"
    "ds_read_b64_tr_b16 a[30:31], v5 offset:6656\n"
    "s_waitcnt lgkmcnt(0)\n"
    "s_add_u32 m0, s62, 4096\n"
    "s_add_u32 s79, s79, 1\n"
    "s_cmp_eq_u32 s79, 7\n"
    "s_cselect_b32 s79, 0, s79\n"
    "s_mul_i32 s80, s79, 262144\n"
    "s_add_u32 s80, s80, s78\n"
    "buffer_load_dwordx4 v3, s[96:99], s80 offen  lds\n"
    "s_add_u32 m0, s62, 5120\n"
    "s_add_u32 s80, s80, 16384\n"
    "buffer_load_dwordx4 v3, s[96:99], s80 offen  lds\n"
    "s_add_u32 m0, s62, 6144\n"
    "s_add_u32 s80, s80, 49152\n"
    "buffer_load_dwordx4 v3, s[96:99], s80 offen  lds\n"
    "s_add_u32 m0, s62, 7168\n"
    "s_add_u32 s80, s80, 16384\n"
    "buffer_load_dwordx4 v3, s[96:99], s80 offen  lds\n"
    "s_waitcnt vmcnt(26)\n"
    "ds_read_b64_tr_b16 a[32:33], v4 offset:8192\n"
    "ds_read_b64_tr_b16 a[34:35], v4 offset:8704\n"
    "ds_read_b64_tr_b16 a[36:37], v5 offset:8192\n"
    "ds_read_b64_tr_b16 a[38:39], v5 offset:8704\n"
    "s_waitcnt vmcnt(24)\n"
    "ds_read_b64_tr_b16 a[40:41], v4 offset:10240\n"
    "ds_read_b64_tr_b16 a[42:43], v4 offset:10752\n"
    "ds_read_b64_tr_b16 a[44:45], v5 offset:10240\n"
    "ds_read_b64_tr_b16 a[46:47], v5 offset:10752\n"
    "s_waitcnt lgkmcnt(0)\n"
    "s_add_u32 m0, s62, 8192\n"
    "s_add_u32 s80, s80, 49152\n"
    "buffer_load_dwordx4 v3, s[96:99], s80 offen  lds\n"
    "s_add_u32 m0, s62, 9216\n"
    "s_add_u32 s80, s80, 16384\n"
    "buffer_load_dwordx4 v3, s[96:99], s80 offen  lds\n"
    "s_add_u32 m0, s62, 10240\n"
    "s_add_u32 s80, s80, 49152\n"
    "buffer_load_dwordx4 v3, s[96:99], s80 offen  lds\n"
    "s_add_u32 m0, s62, 11264\n"
    "s_add_u32 s80, s80, 16384\n"
    "buffer_load_dwordx4 v3, s[96:99], s80 offen  lds\n"
    "s_waitcnt vmcnt(26)\n"
    "ds_read_b64_tr_b16 a[48:49], v4 offset:12288\n"
    "ds_read_b64_tr_b16 a[50:51], v4 offset:12800\n"
    "ds_read_b64_tr_b16 a[52:53], v5 offset:12288\n"
    "ds_read_b64_tr_b16 a[54:55], v5 offset:12800\n"
    "s_waitcnt vmcnt(24)\n"
    "ds_read_b64_tr_b16 a[56:57], v4 offset:14336\n"
    "ds_read_b64_tr_b16 a[58:59], v4 offset:14848\n"
    "ds_read_b64_tr_b16 a[60:61], v5 offset:14336\n"
    "ds_read_b64_tr_b16 a[62:63], v5 offset:14848\n"
    "s_waitcnt lgkmcnt(0)\n"
    "s_add_u32 m0, s62, 12288\n"
    "s_add_u32 s79, s79, 1\n"
    "s_cmp_eq_u32 s79, 7\n"
    "s_cselect_b32 s79, 0, s79\n"
    "s_mul_i32 s80, s79, 262144\n"
    "s_add_u32 s80, s80, s78\n"
    "buffer_load_dwordx4 v3, s[96:99], s80 offen  lds\n"
    "s_add_u32 m0, s62, 13312\n"
    "s_add_u32 s80, s80, 16384\n"
    "buffer_load_dwordx4 v3, s[96:99], s80 offen  lds\n"
    "s_add_u32 m0, s62, 14336\n"
    "s_add_u32 s80, s80, 49152\n"
    "buffer_load_dwordx4 v3, s[96:99], s80 offen  lds\n"
    "s_add_u32 m0, s62, 15360\n"
    "s_add_u32 s80, s80, 16384\n"
    "buffer_load_dwordx4 v3, s[96:99], s80 offen  lds\n"
    "s_waitcnt vmcnt(26)\n"
    "ds_read_b64_tr_b16 a[64:65], v4 offset:16384\n"
    "ds_read_b64_tr_b16 a[66:67], v4 offset:16896\n"
    "ds_read_b64_tr_b16 a[68:69], v5 offset:16384\n"
    "ds_read_b64_tr_b16 a[70:71], v5 offset:16896\n"
    "s_waitcnt vmcnt(24)\n"
    "ds_read_b64_tr_b16 a[72:73], v4 offset:18432\n"
    "ds_read_b64_tr_b16 a[74:75], v4 offset:18944\n"
    "ds_read_b64_tr_b16 a[76:77], v5 offset:18432\n"
    "ds_read_b64_tr_b16 a[78:79], v5 offset:18944\n"
    "s_waitcnt lgkmcnt(0)\n"
    "s_add_u32 m0, s62, 16384\n"
    "s_add_u32 s80, s80, 49152\n"
    "buffer_load_dwordx4 v3, s[96:99], s80 offen  lds\n"
    "s_add_u32 m0, s62, 17408\n"
    "s_add_u32 s80, s80, 16384\n"
    "buffer_load_dwordx4 v3, s[96:99], s80 offen  lds\n"
    "s_add_u32 m0, s62, 18432\n"
    "s_add_u32 s80, s80, 49152\n"
    "buffer_load_dwordx4 v3, s[96:99], s80 offen  lds\n"
    "s_add_u32 m0, s62, 19456\n"
    "s_add_u32 s80, s80, 16384\n"
    "buffer_load_dwordx4 v3, s[96:99], s80 offen  lds\n"
    "s_waitcnt vmcnt(26)\n"
    "ds_read_b64_tr_b16 a[80:81], v4 offset:20480\n"
    "ds_read_b64_tr_b16 a[82:83], v4 offset:20992\n"
    "ds_read_b64_tr_b16 a[84:85], v5 offset:20480\n"
    "ds_read_b64_tr_b16 a[86:87], v5 offset:20992\n"
    "s_waitcnt vmcnt(24)\n"
    "ds_read_b64_tr_b16 a[88:89], v4 offset:22528\n"
    "ds_read_b64_tr_b16 a[90:91], v4 offset:23040\n"
    "ds_read_b64_tr_b16 a[92:93], v5 offset:22528\n"
    "ds_read_b64_tr_b16 a[94:95], v5 offset:23040\n"
    "s_waitcnt lgkmcnt(0)\n"
    "s_add_u32 m0, s62, 20480\n"
    "s_add_u32 s79, s79, 1\n"
    "s_cmp_eq_u32 s79, 7\n"
    "s_cselect_b32 s79, 0, s79\n"
    "s_mul_i32 s80, s79, 262144\n"
    "s_add_u32 s80, s80, s78\n"
    "buffer_load_dwordx4 v3, s[96:99], s80 offen  lds\n"
    "s_add_u32 m0, s62, 21504\n"
    "s_add_u32 s80, s80, 16384\n"
    "buffer_load_dwordx4 v3, s[96:99], s80 offen  lds\n"
    "s_add_u32 m0, s62, 22528\n"
    "s_add_u32 s80, s80, 49152\n"
    "buffer_load_dwordx4 v3, s[96:99], s80 offen  lds\n"
    "s_add_u32 m0, s62, 23552\n"
    "s_add_u32 s80, s80, 16384\n"
    "buffer_load_dwordx4 v3, s[96:99], s80 offen  lds\n"
    "global_load_dwordx2 v[224:225], v15, s[48:49] sc1\n"
    "s_waitcnt vmcnt(27)\n"
    "ds_read_b64_tr_b16 a[96:97], v4 offset:24576\n"
    "ds_read_b64_tr_b16 a[98:99], v4 offset:25088\n"
    "ds_read_b64_tr_b16 a[100:101], v5 offset:24576\n"
    "ds_read_b64_tr_b16 a[102:103], v5 offset:25088\n"
    "s_waitcnt vmcnt(25)\n"
    "ds_read_b64_tr_b16 a[104:105], v4 offset:26624\n"
    "ds_read_b64_tr_b16 a[106:107], v4 offset:27136\n"
    "ds_read_b64_tr_b16 a[108:109], v5 offset:26624\n"
    "ds_read_b64_tr_b16 a[110:111], v5 offset:27136\n"
    "s_waitcnt lgkmcnt(0)\n"
    "s_add_u32 m0, s62, 24576\n"
    "s_add_u32 s80, s80, 49152\n"
    "buffer_load_dwordx4 v3, s[96:99], s80 offen  lds\n"
    "s_add_u32 m0, s62, 25600\n"
    "s_add_u32 s80, s80, 16384\n"
    "buffer_load_dwordx4 v3, s[96:99], s80 offen  lds\n"
    "s_add_u32 m0, s62, 26624\n"
    "s_add_u32 s80, s80, 49152\n"
    "buffer_load_dwordx4 v3, s[96:99], s80 offen  lds\n"
    "s_add_u32 m0, s62, 27648\n"
    "s_add_u32 s80, s80, 16384\n"
    "buffer_load_dwordx4 v3, s[96:99], s80 offen  lds\n"
    "s_mul_i32 s72, s91, 229376\n"
    "s_mul_i32 s64, s54, 57344\n"
    "s_add_u32 s72, s72, s64\n"
    "s_mov_b32 s73, s82\n"
    "s_waitcnt vmcnt(4)\n"
    "s_mov_b32 s71, 0\n"
    "L_p3_%=:\n"
    "v_cmp_ne_u64 s[64:65], s[50:51], v[224:225]\n"
    "s_nop 1\n"
    "s_cmp_eq_u64 s[64:65], 0\n"
    "s_cbranch_scc1 L_p3_done_%=\n"
    "s_sleep 1\n"
    "global_load_dwordx2 v[224:225], v15, s[48:49] sc1\n"
    "s_add_u32 s71, s71, 1\n"
    "s_cmp_lt_u32 s71, 400000\n"
    "s_waitcnt vmcnt(0)\n"
    "s_cbranch_scc1 L_p3_%=\n"
    "L_p3_done_%=:\n"
    "s_lshl_b32 s63, s73, 13\n"
    "s_add_u32 s63, s63, s72\n"
    "buffer_load_dwordx4 v[96:99], v6, s[40:43], s63 offen offset:0 sc1\n"
    "ds_read_b64_tr_b16 a[112:113], v4 offset:0\n"
    "ds_read_b64_tr_b16 a[114:115], v4 offset:512\n"
    "ds_read_b64_tr_b16 a[116:117], v5 offset:0\n"
    "ds_read_b64_tr_b16 a[118:119], v5 offset:512\n"
    "buffer_load_dwordx4 v[100:103], v7, s[40:43], s63 offen offset:0 sc1\n"
    "ds_read_b64_tr_b16 a[120:121], v4 offset:2048\n"
    "ds_read_b64_tr_b16 a[122:123], v4 offset:2560\n"
    "ds_read_b64_tr_b16 a[124:125], v5 offset:2048\n"
    "ds_read_b64_tr_b16 a[126:127], v5 offset:2560\n"
    "buffer_load_dwordx4 v[104:107], v6, s[40:43], s63 offen offset:2048 sc1\n"
    "ds_read_b64_tr_b16 a[128:129], v4 offset:4096\n"
    "ds_read_b64_tr_b16 a[130:131], v4 offset:4608\n"
    "ds_read_b64_tr_b16 a[132:133], v5 offset:4096\n"
    "ds_read_b64_tr_b16 a[134:135], v5 offset:4608\n"
    "buffer_load_dwordx4 v[108:111], v7, s[40:43], s63 offen offset:2048 sc1\n"
    "ds_read_b64_tr_b16 a[136:137], v4 offset:6144\n"
    "ds_read_b64_tr_b16 a[138:139], v4 offset:6656\n"
    "ds_read_b64_tr_b16 a[140:141], v5 offset:6144\n"
    "ds_read_b64_tr_b16 a[142:143], v5 offset:6656\n"
    "s_add_u32 s63, s63, 4096\n"
    "buffer_load_dwordx4 v[112:115], v6, s[40:43], s63 offen offset:0 sc1\n"
    "ds_read_b64_tr_b16 a[144:145], v4 offset:8192\n"
    "ds_read_b64_tr_b16 a[146:147], v4 offset:8704\n"
    "ds_read_b64_tr_b16 a[148:149], v5 offset:8192\n"
    "ds_read_b64_tr_b16 a[150:151], v5 offset:8704\n"
    "buffer_load_dwordx4 v[116:119], v7, s[40:43], s63 offen offset:0 sc1\n"
    "ds_read_b64_tr_b16 a[152:153], v4 offset:10240\n"
    "ds_read_b64_tr_b16 a[154:155], v4 offset:10752\n"
    "ds_read_b64_tr_b16 a[156:157], v5 offset:10240\n"
    "ds_read_b64_tr_b16 a[158:159], v5 offset:10752\n"
    "buffer_load_dwordx4 v[120:123], v6, s[40:43], s63 offen offset:2048 sc1\n"
    "ds_read_b64_tr_b16 a[160:161], v4 offset:12288\n"
    "ds_read_b64_tr_b16 a[162:163], v4 offset:12800\n"
    "ds_read_b64_tr_b16 a[164:165], v5 offset:12288\n"
    "ds_read_b64_tr_b16 a[166:167], v5 offset:12800\n"
    "buffer_load_dwordx4 v[124:127], v7, s[40:43], s63 offen offset:2048 sc1\n"
    "ds_read_b64_tr_b16 a[168:169], v4 offset:14336\n"
    "ds_read_b64_tr_b16 a[170:171], v4 offset:14848\n"
    "ds_read_b64_tr_b16 a[172:173], v5 offset:14336\n"
    "ds_read_b64_tr_b16 a[174:175], v5 offset:14848\n"
    "s_add_u32 s73, s73, 1\n"
    "s_cmp_eq_u32 s73, 7\n"
    "s_cselect_b32 s73, 0, s73\n"
    "s_lshl_b32 s63, s73, 13\n"
    "s_add_u32 s63, s63, s72\n"
    "buffer_load_dwordx4 v[128:131], v6, s[40:43], s63 offen offset:0 sc1\n"
    "ds_read_b64_tr_b16 a[176:177], v4 offset:16384\n"
    "ds_read_b64_tr_b16 a[178:179], v4 offset:16896\n"
    "ds_read_b64_tr_b16 a[180:181], v5 offset:16384\n"
    "ds_read_b64_tr_b16 a[182:183], v5 offset:16896\n"
    "buffer_load_dwordx4 v[132:135], v7, s[40:43], s63 offen offset:0 sc1\n"
    "ds_read_b64_tr_b16 a[184:185], v4 offset:18432\n"
    "ds_read_b64_tr_b16 a[186:187], v4 offset:18944\n"
    "ds_read_b64_tr_b16 a[188:189], v5 offset:18432\n"
    "ds_read_b64_tr_b16 a[190:191], v5 offset:18944\n"
    "ds_read_b64_tr_b16 a[192:193], v4 offset:20480\n"
    "ds_read_b64_tr_b16 a[194:195], v4 offset:20992\n"
    "ds_read_b64_tr_b16 a[196:197], v5 offset:20480\n"
    "ds_read_b64_tr_b16 a[198:199], v5 offset:20992\n"
    "ds_read_b64_tr_b16 a[200:201], v4 offset:22528\n"
    "ds_read_b64_tr_b16 a[202:203], v4 offset:23040\n"
    "ds_read_b64_tr_b16 a[204:205], v5 offset:22528\n"
    "ds_read_b64_tr_b16 a[206:207], v5 offset:23040\n"
    "s_waitcnt vmcnt(12)\n"
    "ds_read_b64_tr_b16 a[208:209], v4 offset:24576\n"
    "ds_read_b64_tr_b16 a[210:211], v4 offset:25088\n"
    "ds_read_b64_tr_b16 a[212:213], v5 offset:24576\n"
    "ds_read_b64_tr_b16 a[214:215], v5 offset:25088\n"
    "s_waitcnt vmcnt(10)\n"
    "ds_read_b64_tr_b16 a[216:217], v4 offset:26624\n"
    "ds_read_b64_tr_b16 a[218:219], v4 offset:27136\n"
    "ds_read_b64_tr_b16 a[220:221], v5 offset:26624\n"
    "ds_read_b64_tr_b16 a[222:223], v5 offset:27136\n"
    "s_waitcnt lgkmcnt(0)\n"
    "L_gemm2_%=:\n"
    "s_waitcnt vmcnt(9)\n"
    "v_mfma_f32_32x32x16_bf16 v[32:47], v[96:99], a[0:3], v[32:47]\n"
    "v_mfma_f32_32x32x16_bf16 v[64:79], v[96:99], a[4:7], 0\n"
    "buffer_load_dwordx4 v[136:139], v6, s[40:43], s63 offen offset:2048 sc1\n"
    "s_waitcnt vmcnt(9)\n"
    "v_mfma_f32_32x32x16_bf16 v[48:63], v[100:103], a[0:3], 0\n"
    "v_mfma_f32_32x32x16_bf16 v[80:95], v[100:103], a[4:7], 0\n"
    "buffer_load_dwordx4 v[140:143], v7, s[40:43], s63 offen offset:2048 sc1\n"
    "s_waitcnt vmcnt(9)\n"
    "v_mfma_f32_32x32x16_bf16 v[32:47], v[104:107], a[8:11], v[32:47]\n"
    "v_mfma_f32_32x32x16_bf16 v[64:79], v[104:107], a[12:15], v[64:79]\n"
    "s_add_u32 s63, s63, 4096\n"
    "buffer_load_dwordx4 v[144:147], v6, s[40:43], s63 offen offset:0 sc1\n"
    "s_waitcnt vmcnt(9)\n"
    "v_mfma_f32_32x32x16_bf16 v[48:63], v[108:111], a[8:11], v[48:63]\n"
    "v_mfma_f32_32x32x16_bf16 v[80:95], v[108:111], a[12:15], v[80:95]\n"
    "buffer_load_dwordx4 v[148:151], v7, s[40:43], s63 offen offset:0 sc1\n"
    "s_waitcnt vmcnt(9)\n"
    "v_mfma_f32_32x32x16_bf16 v[32:47], v[112:115], a[16:19], v[32:47]\n"
    "v_mfma_f32_32x32x16_bf16 v[64:79], v[112:115], a[20:23], v[64:79]\n"
    "buffer_load_dwordx4 v[152:155], v6, s[40:43], s63 offen offset:2048 sc1\n"
    "s_waitcnt vmcnt(9)\n"
    "v_mfma_f32_32x32x16_bf16 v[48:63], v[116:119], a[16:19], v[48:63]\n"
    "v_mfma_f32_32x32x16_bf16 v[80:95], v[116:119], a[20:23], v[80:95]\n"
    "buffer_load_dwordx4 v[156:159], v7, s[40:43], s63 offen offset:2048 sc1\n"
    "s_waitcnt vmcnt(9)\n"
    "v_mfma_f32_32x32x16_bf16 v[32:47], v[120:123], a[24:27], v[32:47]\n"
    "v_mfma_f32_32x32x16_bf16 v[64:79], v[120:123], a[28:31], v[64:79]\n"
    "s_add_u32 s73, s73, 1\n"
    "s_cmp_eq_u32 s73, 7\n"
    "s_cselect_b32 s73, 0, s73\n"
    "s_lshl_b32 s63, s73, 13\n"
    "s_add_u32 s63, s63, s72\n"
    "buffer_load_dwordx4 v[160:163], v6, s[40:43], s63 offen offset:0 sc1\n"
    "s_waitcnt vmcnt(9)\n"
    "v_mfma_f32_32x32x16_bf16 v[48:63], v[124:127], a[24:27], v[48:63]\n"
    "v_mfma_f32_32x32x16_bf16 v[80:95], v[124:127], a[28:31], v[80:95]\n"
    "buffer_load_dwordx4 v[164:167], v7, s[40:43], s63 offen offset:0 sc1\n"
    "s_waitcnt vmcnt(9)\n"
    "v_mfma_f32_32x32x16_bf16 v[32:47], v[128:131], a[32:35], v[32:47]\n"
    "v_mfma_f32_32x32x16_bf16 v[64:79], v[128:131], a[36:39], v[64:79]\n"
    "buffer_load_dwordx4 v[168:171], v6, s[40:43], s63 offen offset:2048 sc1\n"
    "s_waitcnt vmcnt(9)\n"
    "v_mfma_f32_32x32x16_bf16 v[48:63], v[132:135], a[32:35], v[48:63]\n"
    "v_mfma_f32_32x32x16_bf16 v[80:95], v[132:135], a[36:39], v[80:95]\n"
    "buffer_load_dwordx4 v[172:175], v7, s[40:43], s63 offen offset:2048 sc1\n"
    "s_waitcnt vmcnt(9)\n"
    "v_mfma_f32_32x32x16_bf16 v[32:47], v[136:139], a[40:43], v[32:47]\n"
    "v_mfma_f32_32x32x16_bf16 v[64:79], v[136:139], a[44:47], v[64:79]\n"
    "s_add_u32 s63, s63, 4096\n"
    "buffer_load_dwordx4 v[176:179], v6, s[40:43], s63 offen offset:0 sc1\n"
    "s_waitcnt vmcnt(9)\n"
    "v_mfma_f32_32x32x16_bf16 v[48:63], v[140:143], a[40:43], v[48:63]\n"
    "v_mfma_f32_32x32x16_bf16 v[80:95], v[140:143], a[44:47], v[80:95]\n"
    "buffer_load_dwordx4 v[180:183], v7, s[40:43], s63 offen offset:0 sc1\n"
    "s_waitcnt vmcnt(9)\n"
    "v_mfma_f32_32x32x16_bf16 v[32:47], v[144:147], a[48:51], v[32:47]\n"
    "v_mfma_f32_32x32x16_bf16 v[64:79], v[144:147], a[52:55], v[64:79]\n"
    "buffer_load_dwordx4 v[184:187], v6, s[40:43], s63 offen offset:2048 sc1\n"
    "s_waitcnt vmcnt(9)\n"
    "v_mfma_f32_32x32x16_bf16 v[48:63], v[148:151], a[48:51], v[48:63]\n"
    "v_mfma_f32_32x32x16_bf16 v[80:95], v[148:151], a[52:55], v[80:95]\n"
    "buffer_load_dwordx4 v[188:191], v7, s[40:43], s63 offen offset:2048 sc1\n"
    "s_waitcnt vmcnt(9)\n"
    "v_mfma_f32_32x32x16_bf16 v[32:47], v[152:155], a[56:59], v[32:47]\n"
    "v_mfma_f32_32x32x16_bf16 v[64:79], v[152:155], a[60:63], v[64:79]\n"
    "s_add_u32 s73, s73, 1\n"
    "s_cmp_eq_u32 s73, 7\n"
    "s_cselect_b32 s73, 0, s73\n"
    "s_lshl_b32 s63, s73, 13\n"
    "s_add_u32 s63, s63, s72\n"
    "buffer_load_dwordx4 v[192:195], v6, s[40:43], s63 offen offset:0 sc1\n"
    "s_waitcnt vmcnt(9)\n"
    "v_mfma_f32_32x32x16_bf16 v[48:63], v[156:159], a[56:59], v[48:63]\n"
    "v_mfma_f32_32x32x16_bf16 v[80:95], v[156:159], a[60:63], v[80:95]\n"
    "buffer_load_dwordx4 v[196:199], v7, s[40:43], s63 offen offset:0 sc1\n"
    "s_waitcnt vmcnt(9)\n"
    "v_mfma_f32_32x32x16_bf16 v[32:47], v[160:163], a[64:67], v[32:47]\n"
    "v_mfma_f32_32x32x16_bf16 v[64:79], v[160:163], a[68:71], v[64:79]\n"
    "buffer_load_dwordx4 v[200:203], v6, s[40:43], s63 offen offset:2048 sc1\n"
    "s_waitcnt vmcnt(9)\n"
    "v_mfma_f32_32x32x16_bf16 v[48:63], v[164:167], a[64:67], v[48:63]\n"
    "v_mfma_f32_32x32x16_bf16 v[80:95], v[164:167], a[68:71], v[80:95]\n"
    "buffer_load_dwordx4 v[204:207], v7, s[40:43], s63 offen offset:2048 sc1\n"
    "s_waitcnt vmcnt(9)\n"
    "v_mfma_f32_32x32x16_bf16 v[32:47], v[168:171], a[72:75], v[32:47]\n"
    "v_mfma_f32_32x32x16_bf16 v[64:79], v[168:171], a[76:79], v[64:79]\n"
    "s_add_u32 s63, s63, 4096\n"
    "buffer_load_dwordx4 v[96:99], v6, s[40:43], s63 offen offset:0 sc1\n"
    "s_waitcnt vmcnt(9)\n"
    "v_mfma_f32_32x32x16_bf16 v[48:63], v[172:175], a[72:75], v[48:63]\n"
    "v_mfma_f32_32x32x16_bf16 v[80:95], v[172:175], a[76:79], v[80:95]\n"
    "buffer_load_dwordx4 v[100:103], v7, s[40:43], s63 offen offset:0 sc1\n"
    "s_waitcnt vmcnt(9)\n"
    "v_mfma_f32_32x32x16_bf16 v[32:47], v[176:179], a[80:83], v[32:47]\n"
    "v_mfma_f32_32x32x16_bf16 v[64:79], v[176:179], a[84:87], v[64:79]\n"
    "buffer_load_dwordx4 v[104:107], v6, s[40:43], s63 offen offset:2048 sc1\n"
    "s_waitcnt vmcnt(9)\n"
    "v_mfma_f32_32x32x16_bf16 v[48:63], v[180:183], a[80:83], v[48:63]\n"
    "v_mfma_f32_32x32x16_bf16 v[80:95], v[180:183], a[84:87], v[80:95]\n"
    "buffer_load_dwordx4 v[108:111], v7, s[40:43], s63 offen offset:2048 sc1\n"
    "s_waitcnt vmcnt(9)\n"
    "v_mfma_f32_32x32x16_bf16 v[32:47], v[184:187], a[88:91], v[32:47]\n"
    "v_mfma_f32_32x32x16_bf16 v[64:79], v[184:187], a[92:95], v[64:79]\n"
    "s_add_u32 s73, s73, 1\n"
    "s_cmp_eq_u32 s73, 7\n"
    "s_cselect_b32 s73, 0, s73\n"
    "s_lshl_b32 s63, s73, 13\n"
    "s_add_u32 s63, s63, s72\n"
    "buffer_load_dwordx4 v[112:115], v6, s[40:43], s63 offen offset:0 sc1\n"
    "s_waitcnt vmcnt(9)\n"
    "v_mfma_f32_32x32x16_bf16 v[48:63], v[188:191], a[88:91], v[48:63]\n"
    "v_mfma_f32_32x32x16_bf16 v[80:95], v[188:191], a[92:95], v[80:95]\n"
    "buffer_load_dwordx4 v[116:119], v7, s[40:43], s63 offen offset:0 sc1\n"
    "s_waitcnt vmcnt(9)\n"
    "v_mfma_f32_32x32x16_bf16 v[32:47], v[192:195], a[96:99], v[32:47]\n"
    "v_mfma_f32_32x32x16_bf16 v[64:79], v[192:195], a[100:103], v[64:79]\n"
    "buffer_load_dwordx4 v[120:123], v6, s[40:43], s63 offen offset:2048 sc1\n"
    "s_waitcnt vmcnt(9)\n"
    "v_mfma_f32_32x32x16_bf16 v[48:63], v[196:199], a[96:99], v[48:63]\n"
    "v_mfma_f32_32x32x16_bf16 v[80:95], v[196:199], a[100:103], v[80:95]\n"
    "buffer_load_dwordx4 v[124:127], v7, s[40:43], s63 offen offset:2048 sc1\n"
    "s_waitcnt vmcnt(9)\n"
    "v_mfma_f32_32x32x16_bf16 v[32:47], v[200:203], a[104:107], v[32:47]\n"
    "v_mfma_f32_32x32x16_bf16 v[64:79], v[200:203], a[108:111], v[64:79]\n"
    "s_add_u32 s63, s63, 4096\n"
    "buffer_load_dwordx4 v[128:131], v6, s[40:43], s63 offen offset:0 sc1\n"
    "s_waitcnt vmcnt(9)\n"
    "v_mfma_f32_32x32x16_bf16 v[48:63], v[204:207], a[104:107], v[48:63]\n"
    "v_mfma_f32_32x32x16_bf16 v[80:95], v[204:207], a[108:111], v[80:95]\n"
    "buffer_load_dwordx4 v[132:135], v7, s[40:43], s63 offen offset:0 sc1\n"
    "s_waitcnt vmcnt(9)\n"
    "v_mfma_f32_32x32x16_bf16 v[32:47], v[96:99], a[112:115], v[32:47]\n"
    "v_mfma_f32_32x32x16_bf16 v[64:79], v[96:99], a[116:119], v[64:79]\n"
    "buffer_load_dwordx4 v[136:139], v6, s[40:43], s63 offen offset:2048 sc1\n"
    "s_waitcnt vmcnt(9)\n"
    "v_mfma_f32_32x32x16_bf16 v[48:63], v[100:103], a[112:115], v[48:63]\n"
    "v_mfma_f32_32x32x16_bf16 v[80:95], v[100:103], a[116:119], v[80:95]\n"
    "buffer_load_dwordx4 v[140:143], v7, s[40:43], s63 offen offset:2048 sc1\n"
    "s_waitcnt vmcnt(9)\n"
    "v_mfma_f32_32x32x16_bf16 v[32:47], v[104:107], a[120:123], v[32:47]\n"
    "v_mfma_f32_32x32x16_bf16 v[64:79], v[104:107], a[124:127], v[64:79]\n"
    "s_add_u32 s73, s73, 1\n"
    "s_cmp_eq_u32 s73, 7\n"
    "s_cselect_b32 s73, 0, s73\n"
    "s_lshl_b32 s63, s73, 13\n"
    "s_add_u32 s63, s63, s72\n"
    "buffer_load_dwordx4 v[144:147], v6, s[40:43], s63 offen offset:0 sc1\n"
    "s_waitcnt vmcnt(9)\n"
    "v_mfma_f32_32x32x16_bf16 v[48:63], v[108:111], a[120:123], v[48:63]\n"
    "v_mfma_f32_32x32x16_bf16 v[80:95], v[108:111], a[124:127], v[80:95]\n"
    "buffer_load_dwordx4 v[148:151], v7, s[40:43], s63 offen offset:0 sc1\n"
    "s_waitcnt vmcnt(9)\n"
    "v_mfma_f32_32x32x16_bf16 v[32:47], v[112:115], a[128:131], v[32:47]\n"
    "v_mfma_f32_32x32x16_bf16 v[64:79], v[112:115], a[132:135], v[64:79]\n"
    "buffer_load_dwordx4 v[152:155], v6, s[40:43], s63 offen offset:2048 sc1\n"
    "s_waitcnt vmcnt(9)\n"
    "v_mfma_f32_32x32x16_bf16 v[48:63], v[116:119], a[128:131], v[48:63]\n"
    "v_mfma_f32_32x32x16_bf16 v[80:95], v[116:119], a[132:135], v[80:95]\n"
    "buffer_load_dwordx4 v[156:159], v7, s[40:43], s63 offen offset:2048 sc1\n"
    "s_waitcnt vmcnt(9)\n"
    "v_mfma_f32_32x32x16_bf16 v[32:47], v[120:123], a[136:139], v[32:47]\n"
    "v_mfma_f32_32x32x16_bf16 v[64:79], v[120:123], a[140:143], v[64:79]\n"
    "s_add_u32 s63, s63, 4096\n"
    "buffer_load_dwordx4 v[160:163], v6, s[40:43], s63 offen offset:0 sc1\n"
    "s_waitcnt vmcnt(9)\n"
    "v_mfma_f32_32x32x16_bf16 v[48:63], v[124:127], a[136:139], v[48:63]\n"
    "v_mfma_f32_32x32x16_bf16 v[80:95], v[124:127], a[140:143], v[80:95]\n"
    "buffer_load_dwordx4 v[164:167], v7, s[40:43], s63 offen offset:0 sc1\n"
    "s_waitcnt vmcnt(9)\n"
    "v_mfma_f32_32x32x16_bf16 v[32:47], v[128:131], a[144:147], v[32:47]\n"
    "v_mfma_f32_32x32x16_bf16 v[64:79], v[128:131], a[148:151], v[64:79]\n"
    "buffer_load_dwordx4 v[168:171], v6, s[40:43], s63 offen offset:2048 sc1\n"
    "s_waitcnt vmcnt(9)\n"
    "v_mfma_f32_32x32x16_bf16 v[48:63], v[132:135], a[144:147], v[48:63]\n"
    "v_mfma_f32_32x32x16_bf16 v[80:95], v[132:135], a[148:151], v[80:95]\n"
    "buffer_load_dwordx4 v[172:175], v7, s[40:43], s63 offen offset:2048 sc1\n"
    "s_waitcnt vmcnt(9)\n"
    "v_mfma_f32_32x32x16_bf16 v[32:47], v[136:139], a[152:155], v[32:47]\n"
    "v_mfma_f32_32x32x16_bf16 v[64:79], v[136:139], a[156:159], v[64:79]\n"
    "s_add_u32 s73, s73, 1\n"
    "s_cmp_eq_u32 s73, 7\n"
    "s_cselect_b32 s73, 0, s73\n"
    "s_lshl_b32 s63, s73, 13\n"
    "s_add_u32 s63, s63, s72\n"
    "s_add_u32 s83, s63, 4096\n"
    "buffer_load_dwordx4 v[180:183], v7, s[40:43], s63 offen offset:0 sc1\n"
    "s_waitcnt vmcnt(9)\n"
    "v_mfma_f32_32x32x16_bf16 v[48:63], v[140:143], a[152:155], v[48:63]\n"
    "v_mfma_f32_32x32x16_bf16 v[80:95], v[140:143], a[156:159], v[80:95]\n"
    "buffer_load_dwordx4 v[188:191], v7, s[40:43], s63 offen offset:2048 sc1\n"
    "s_waitcnt vmcnt(9)\n"
    "v_mfma_f32_32x32x16_bf16 v[32:47], v[144:147], a[160:163], v[32:47]\n"
    "v_mfma_f32_32x32x16_bf16 v[64:79], v[144:147], a[164:167], v[64:79]\n"
    "buffer_load_dwordx4 v[196:199], v7, s[40:43], s83 offen offset:0 sc1\n"
    "s_waitcnt vmcnt(9)\n"
    "v_mfma_f32_32x32x16_bf16 v[48:63], v[148:151], a[160:163], v[48:63]\n"
    "v_mfma_f32_32x32x16_bf16 v[80:95], v[148:151], a[164:167], v[80:95]\n"
    "buffer_load_dwordx4 v[204:207], v7, s[40:43], s83 offen offset:2048 sc1\n"
    "s_waitcnt vmcnt(9)\n"
    "v_mfma_f32_32x32x16_bf16 v[32:47], v[152:155], a[168:171], v[32:47]\n"
    "v_mfma_f32_32x32x16_bf16 v[64:79], v[152:155], a[172:175], v[64:79]\n"
    "buffer_load_dwordx4 v[176:179], v6, s[40:43], s63 offen offset:0 sc1\n"
    "s_waitcnt vmcnt(9)\n"
    "v_mfma_f32_32x32x16_bf16 v[48:63], v[156:159], a[168:171], v[48:63]\n"
    "v_mfma_f32_32x32x16_bf16 v[80:95], v[156:159], a[172:175], v[80:95]\n"
    "buffer_load_dwordx4 v[184:187], v6, s[40:43], s63 offen offset:2048 sc1\n"
    "s_waitcnt vmcnt(9)\n"
    "v_mfma_f32_32x32x16_bf16 v[32:47], v[160:163], a[176:179], v[32:47]\n"
    "v_mfma_f32_32x32x16_bf16 v[64:79], v[160:163], a[180:183], v[64:79]\n"
    "buffer_load_dwordx4 v[192:195], v6, s[40:43], s83 offen offset:0 sc1\n"
    "s_waitcnt vmcnt(9)\n"
    "v_mfma_f32_32x32x16_bf16 v[48:63], v[164:167], a[176:179], v[48:63]\n"
    "v_mfma_f32_32x32x16_bf16 v[80:95], v[164:167], a[180:183], v[80:95]\n"
    "buffer_load_dwordx4 v[200:203], v6, s[40:43], s83 offen offset:2048 sc1\n"
    "s_waitcnt vmcnt(9)\n"
    "v_mfma_f32_32x32x16_bf16 v[32:47], v[168:171], a[184:187], v[32:47]\n"
    "v_mfma_f32_32x32x16_bf16 v[64:79], v[168:171], a[188:191], v[64:79]\n"
    "s_waitcnt vmcnt(8)\n"
    "v_mfma_f32_32x32x16_bf16 v[48:63], v[172:175], a[184:187], v[48:63]\n"
    "v_mfma_f32_32x32x16_bf16 v[80:95], v[172:175], a[188:191], v[80:95]\n"
    "s_waitcnt vmcnt(7)\n"
    "v_mfma_f32_32x32x16_bf16 v[48:63], v[180:183], a[192:195], v[48:63]\n"
    "v_mfma_f32_32x32x16_bf16 v[80:95], v[180:183], a[196:199], v[80:95]\n"
    "s_waitcnt vmcnt(6)\n"
    "v_mfma_f32_32x32x16_bf16 v[48:63], v[188:191], a[200:203], v[48:63]\n"
    "v_mfma_f32_32x32x16_bf16 v[80:95], v[188:191], a[204:207], v[80:95]\n"
    "s_waitcnt vmcnt(5)\n"
    "v_mfma_f32_32x32x16_bf16 v[48:63], v[196:199], a[208:211], v[48:63]\n"
    "v_mfma_f32_32x32x16_bf16 v[80:95], v[196:199], a[212:215], v[80:95]\n"
    "s_waitcnt vmcnt(4)\n"
    "v_mfma_f32_32x32x16_bf16 v[48:63], v[204:207], a[216:219], v[48:63]\n"
    "v_mfma_f32_32x32x16_bf16 v[80:95], v[204:207], a[220:223], v[80:95]\n"
    "s_waitcnt vmcnt(3)\n"
    "v_mfma_f32_32x32x16_bf16 v[64:79], v[176:179], a[196:199], v[64:79]\n"
    "s_waitcnt vmcnt(2)\n"
    "v_mfma_f32_32x32x16_bf16 v[64:79], v[184:187], a[204:207], v[64:79]\n"
    "s_waitcnt vmcnt(1)\n"
    "v_mfma_f32_32x32x16_bf16 v[64:79], v[192:195], a[212:215], v[64:79]\n"
    "s_waitcnt vmcnt(0)\n"
    "v_mfma_f32_32x32x16_bf16 v[64:79], v[200:203], a[220:223], v[64:79]\n"
    "s_nop 2\n"
    "ds_write_b128 v8, v[48:51] offset:0\n"
    "ds_write_b128 v8, v[52:55] offset:1024\n"
    "ds_write_b128 v8, v[56:59] offset:2048\n"
    "ds_write_b128 v8, v[60:63] offset:3072\n"
    "v_mfma_f32_32x32x16_bf16 v[32:47], v[176:179], a[192:195], v[32:47]\n"
    "v_mfma_f32_32x32x16_bf16 v[32:47], v[184:187], a[200:203], v[32:47]\n"
    "v_mfma_f32_32x32x16_bf16 v[32:47], v[192:195], a[208:211], v[32:47]\n"
    "v_mfma_f32_32x32x16_bf16 v[32:47], v[200:203], a[216:219], v[32:47]\n"
    "ds_write_b128 v10, v[80:83] offset:0\n"
    "ds_write_b128 v10, v[84:87] offset:1024\n"
    "ds_write_b128 v10, v[88:91] offset:2048\n"
    "ds_write_b128 v10, v[92:95] offset:3072\n"
    "ds_write_b128 v9, v[64:67] offset:0\n"
    "ds_write_b128 v9, v[68:71] offset:1024\n"
    "ds_write_b128 v9, v[72:75] offset:2048\n"
    "ds_write_b128 v9, v[76:79] offset:3072\n"
    "s_waitcnt lgkmcnt(0)\n"
    "s_barrier\n"
    "ds_read_b128 v[96:99], v11 offset:0\n"
    "ds_read_b128 v[100:103], v11 offset:1024\n"
    "ds_read_b128 v[104:107], v11 offset:2048\n"
    "ds_read_b128 v[108:111], v11 offset:3072\n"
    "ds_read_b128 v[112:115], v11 offset:4096\n"
    "ds_read_b128 v[116:119], v11 offset:5120\n"
    "ds_read_b128 v[120:123], v11 offset:6144\n"
    "ds_read_b128 v[124:127], v11 offset:7168\n"
    "ds_read_b128 v[128:131], v11 offset:8192\n"
    "ds_read_b128 v[132:135], v11 offset:9216\n"
    "ds_read_b128 v[136:139], v11 offset:10240\n"
    "ds_read_b128 v[140:143], v11 offset:11264\n"
    "s_waitcnt lgkmcnt(8)\n"
    "v_pk_add_f32 v[32:33], v[32:33], v[96:97]\n"
    "v_pk_add_f32 v[34:35], v[34:35], v[98:99]\n"
    "v_pk_add_f32 v[36:37], v[36:37], v[100:101]\n"
    "v_pk_add_f32 v[38:39], v[38:39], v[102:103]\n"
    "v_pk_add_f32 v[40:41], v[40:41], v[104:105]\n"
    "v_pk_add_f32 v[42:43], v[42:43], v[106:107]\n"
    "v_pk_add_f32 v[44:45], v[44:45], v[108:109]\n"
    "v_pk_add_f32 v[46:47], v[46:47], v[110:111]\n"
    "s_waitcnt lgkmcnt(4)\n"
    "v_pk_add_f32 v[32:33], v[32:33], v[112:113]\n"
    "v_pk_add_f32 v[34:35], v[34:35], v[114:115]\n"
    "v_pk_add_f32 v[36:37], v[36:37], v[116:117]\n"
    "v_pk_add_f32 v[38:39], v[38:39], v[118:119]\n"
    "v_pk_add_f32 v[40:41], v[40:41], v[120:121]\n"
    "v_pk_add_f32 v[42:43], v[42:43], v[122:123]\n"
    "v_pk_add_f32 v[44:45], v[44:45], v[124:125]\n"
    "v_pk_add_f32 v[46:47], v[46:47], v[126:127]\n"
    "s_waitcnt lgkmcnt(0)\n"
    "v_pk_add_f32 v[32:33], v[32:33], v[128:129]\n"
    "v_pk_add_f32 v[34:35], v[34:35], v[130:131]\n"
    "v_pk_add_f32 v[36:37], v[36:37], v[132:133]\n"
    "v_pk_add_f32 v[38:39], v[38:39], v[134:135]\n"
    "v_pk_add_f32 v[40:41], v[40:41], v[136:137]\n"
    "v_pk_add_f32 v[42:43], v[42:43], v[138:139]\n"
    "v_pk_add_f32 v[44:45], v[44:45], v[140:141]\n"
    "v_pk_add_f32 v[46:47], v[46:47], v[142:143]\n"
    "v_cvt_pk_bf16_f32 v144, v32, v33\n"
    "v_cvt_pk_bf16_f32 v145, v34, v35\n"
    "v_cvt_pk_bf16_f32 v146, v36, v37\n"
    "v_cvt_pk_bf16_f32 v147, v38, v39\n"
    "v_cvt_pk_bf16_f32 v148, v40, v41\n"
    "v_cvt_pk_bf16_f32 v149, v42, v43\n"
    "v_cvt_pk_bf16_f32 v150, v44, v45\n"
    "v_cvt_pk_bf16_f32 v151, v46, v47\n"
    "s_lshl_b32 s64, s57, 1\n"
    "s_lshr_b32 s65, s54, 1\n"
    "s_add_u32 s64, s64, s65\n"
    "s_lshl_b32 s64, s64, 15\n"
    "s_lshl_b32 s65, s91, 1\n"
    "s_and_b32 s66, s54, 1\n"
    "s_add_u32 s65, s65, s66\n"
    "s_lshl_b32 s65, s65, 6\n"
    "s_add_u32 s64, s64, s65\n"
    "v_permlane32_swap_b32 v144, v146\n"
    "v_permlane32_swap_b32 v145, v147\n"
    "v_permlane32_swap_b32 v148, v150\n"
    "v_permlane32_swap_b32 v149, v151\n"
    "buffer_store_dwordx4 v[144:147], v13, s[44:47], s64 offen offset:0 \n"
    "buffer_store_dwordx4 v[148:151], v13, s[44:47], s64 offen offset:32 \n"
    "s_or_b32 s64, s70, s54\n"
    "s_cmp_eq_u32 s64, 0\n"
    "s_cbranch_scc0 L_noE_%=\n"
    "v_mov_b32 v22, s52\n"
    "v_mov_b32 v23, s53\n"
    "v_mov_b32 v24, 0\n"
    "s_mov_b64 exec, 1\n"
    "global_store_dwordx2 v24, v[22:23], s[48:49] sc1\n"
    "s_mov_b64 exec, -1\n"
    "L_noE_%=:\n"
    "L_diag_end_%=:\n"
    "L_end_%=:\n"
    "s_nop 0\n"
    :
    : "s"(__builtin_amdgcn_kernarg_segment_ptr()), "s"(blockIdx.x), "v"(threadIdx.x), "s"(lds)
    : "memory", "vcc", "scc", "s8","s9","s10","s11","s12","s13","s14","s15","s16","s17","s18","s19","s20","s21","s22","s23","s24","s25","s26","s27","s28","s29","s30","s31","s36","s37","s38","s39","s40","s41","s42","s43","s44","s45","s46","s47","s48","s49","s50","s51","s52","s53","s54","s55","s56","s57","s58","s59","s60","s61","s62","s63","s64","s65","s66","s67","s68","s69","s70","s71","s72","s73","s74","s75","s76","s77","s78","s79","s80","s81","s82","s83","s84","s85","s86","s87","s88","s89","s90","s91","s92","s93","s94","s95","s96","s97","s98","s99",
      "v1","v2","v3","v4","v5","v6","v7","v8","v9","v10","v11","v12","v13","v14","v15","v16","v17","v18","v19","v20","v21","v22","v23","v24","v25","v26","v27","v28","v29","v30","v31","v32","v33","v34","v35","v36","v37","v38","v39","v40","v41","v42","v43","v44","v45","v46","v47","v48","v49","v50","v51","v52","v53","v54","v55","v56","v57","v58","v59","v60","v61","v62","v63","v64","v65","v66","v67","v68","v69","v70","v71","v72","v73","v74","v75","v76","v77","v78","v79","v80","v81","v82","v83","v84","v85","v86","v87","v88","v89","v90","v91","v92","v93","v94","v95","v96","v97","v98","v99","v100","v101","v102","v103","v104","v105","v106","v107","v108","v109","v110","v111","v112","v113","v114","v115","v116","v117","v118","v119","v120","v121","v122","v123","v124","v125","v126","v127","v128","v129","v130","v131","v132","v133","v134","v135","v136","v137","v138","v139","v140","v141","v142","v143","v144","v145","v146","v147","v148","v149","v150","v151","v152","v153","v154","v155","v156","v157","v158","v159","v160","v161","v162","v163","v164","v165","v166","v167","v168","v169","v170","v171","v172","v173","v174","v175","v176","v177","v178","v179","v180","v181","v182","v183","v184","v185","v186","v187","v188","v189","v190","v191","v192","v193","v194","v195","v196","v197","v198","v199","v200","v201","v202","v203","v204","v205","v206","v207","v208","v209","v210","v211","v212","v213","v214","v215","v216","v217","v218","v219","v220","v221","v222","v223","v224","v225","v226","v227","v228","v229","v230","v231","v232","v233","v234","v235","v236","v237","v238","v239","v240","v241","v242","v243","v244","v245","v246","v247","v248","v249","v250","v251","v252","v253","v254","v255",
      "a0","a1","a2","a3","a4","a5","a6","a7","a8","a9","a10","a11","a12","a13","a14","a15","a16","a17","a18","a19","a20","a21","a22","a23","a24","a25","a26","a27","a28","a29","a30","a31","a32","a33","a34","a35","a36","a37","a38","a39","a40","a41","a42","a43","a44","a45","a46","a47","a48","a49","a50","a51","a52","a53","a54","a55","a56","a57","a58","a59","a60","a61","a62","a63","a64","a65","a66","a67","a68","a69","a70","a71","a72","a73","a74","a75","a76","a77","a78","a79","a80","a81","a82","a83","a84","a85","a86","a87","a88","a89","a90","a91","a92","a93","a94","a95","a96","a97","a98","a99","a100","a101","a102","a103","a104","a105","a106","a107","a108","a109","a110","a111","a112","a113","a114","a115","a116","a117","a118","a119","a120","a121","a122","a123","a124","a125","a126","a127","a128","a129","a130","a131","a132","a133","a134","a135","a136","a137","a138","a139","a140","a141","a142","a143","a144","a145","a146","a147","a148","a149","a150","a151","a152","a153","a154","a155","a156","a157","a158","a159","a160","a161","a162","a163","a164","a165","a166","a167","a168","a169","a170","a171","a172","a173","a174","a175","a176","a177","a178","a179","a180","a181","a182","a183","a184","a185","a186","a187","a188","a189","a190","a191","a192","a193","a194","a195","a196","a197","a198","a199","a200","a201","a202","a203","a204","a205","a206","a207","a208","a209","a210","a211","a212","a213","a214","a215","a216","a217","a218","a219","a220","a221","a222","a223","a224","a225","a226","a227","a228","a229","a230","a231","a232","a233","a234","a235","a236","a237","a238","a239","a240","a241","a242","a243","a244","a245","a246","a247","a248","a249","a250","a251","a252","a253","a254","a255");
}

extern "C" void kernel_launch(void* const* d_in, const int* in_sizes, int n_in, void* d_out, int out_size, void* d_ws, size_t ws_size,
                              hipStream_t stream) {
  (void)in_sizes; (void)n_in; (void)out_size; (void)ws_size;
  standalone__mixer_token_mlp__prompt_kernel<<<256, 256, 0, stream>>>(d_in[0], d_in[1], d_in[2], d_in[3], d_in[4], d_out, d_ws, (char*)d_ws + (8u << 20));
}
```
